```python
import torch

def _softmax(x, axis=-1):
    return torch.softmax(x, dim=axis)

def _gelu(x):
    return torch.nn.functional.gelu(x, approximate='tanh')

def _silu(x):
    return torch.nn.functional.silu(x)

def _sigmoid(x):
    return torch.sigmoid(x)

def _layernorm(x, axis=-1, eps=1e-05):
    m = x.mean(dim=axis, keepdim=True)
    v = x.var(dim=axis, keepdim=True, unbiased=False)
    return (x - m) / torch.sqrt(v + eps)

def _scatter_add(tgt, idx, src, axis=0):
    return tgt.clone().index_add_(axis, idx, src)

def _segment_sum(src, seg, n):
    out = torch.zeros((n,) + src.shape[1:], dtype=src.dtype)
    return out.index_add_(0, seg, src)

def _pad(x, pw):
    flat = tuple((v for pair in reversed(pw) for v in pair))
    return torch.nn.functional.pad(x, flat)
kernel_name = 'sequential_preLN__memory_aug_recurrent_mem_tr__dense_activation_gelu__prompt'

def setup_inputs():

    _w = lambda a: (lambda t: t.to(torch.bfloat16) if t.is_floating_point() else t)(torch.as_tensor(a))

    def _A_setup():
        B, S, Dm = (1, 2048, 576)
        H, Dh = (32, 128)
        M, seg = (8, 32)
        seg = min(seg, S)
        S = S // seg * seg
        return {'x': _w((lambda *s: torch.randn(*s, dtype=torch.bfloat16) * 0.05)(B, S, Dm)), 'mem0': _w((lambda *s: torch.randn(*s, dtype=torch.bfloat16) * 0.2)(B, M, Dm)), 'Wq': _w((lambda *s: torch.randn(*s, dtype=torch.bfloat16) * 1.6)(Dm, H, Dh) * Dm ** (-0.5)), 'Wk': _w((lambda *s: torch.randn(*s, dtype=torch.bfloat16) * 1.6)(Dm, H, Dh) * Dm ** (-0.5)), 'Wv': _w((lambda *s: torch.randn(*s, dtype=torch.bfloat16) * 0.2)(Dm, H, Dh) * Dm ** (-0.5)), 'Wo': _w((lambda *s: torch.randn(*s, dtype=torch.bfloat16) * 0.2)(H, Dh, Dm) * (H * Dh) ** (-0.5)), 'seg_len': seg}
    _A_in = _A_setup()

    def _B_setup():
        B, S, D, F = (1, 2048, 576, 4608)
        return {'x': _w((lambda *s: torch.randn(*s, dtype=torch.bfloat16) * 0.05)(B, S, D)), 'w1': _w((lambda *s: torch.randn(*s, dtype=torch.bfloat16) * 0.4)(D, F) * D ** (-0.5)), 'b1': _w((lambda *s: torch.randn(*s, dtype=torch.bfloat16) * 12.8)(F) * 0.01), 'w2': _w((lambda *s: torch.randn(*s, dtype=torch.bfloat16) * 0.2)(F, D) * F ** (-0.5)), 'b2': _w((lambda *s: torch.randn(*s, dtype=torch.bfloat16) * 3.2)(D) * 0.01)}
    _B_in = _B_setup()
    return {'A_x': _A_in['x'], 'A_mem0': _A_in['mem0'], 'A_Wq': _A_in['Wq'], 'A_Wk': _A_in['Wk'], 'A_Wv': _A_in['Wv'], 'A_Wo': _A_in['Wo'], 'A_seg_len': _A_in['seg_len'], 'B_x': _B_in['x'], 'B_w1': _B_in['w1'], 'B_b1': _B_in['b1'], 'B_w2': _B_in['w2'], 'B_b2': _B_in['b2']}

def reference(A_x, A_mem0, A_Wq, A_Wk, A_Wv, A_Wo, A_seg_len, B_x, B_w1, B_b1, B_w2, B_b2):

    def _A_ref(x, mem0, Wq, Wk, Wv, Wo, seg_len):
        B, S, Dm = x.shape
        H, Dh = (Wq.shape[1], Wq.shape[2])
        M = mem0.shape[1]
        scale = Dh ** (-0.5)
        n_seg = S // seg_len
        L = M + seg_len
        idx = torch.arange(L)
        mask = (idx[:, None] < M) | (idx[:, None] >= idx[None, :])
        mem = mem0
        outs = []
        for s in range(n_seg):
            xs = x[:, s * seg_len:(s + 1) * seg_len, :].clone()
            h = torch.concatenate([mem, xs], axis=1)
            q = torch.einsum('btd,dhe->bhte', h, Wq)
            k = torch.einsum('btd,dhe->bhte', h, Wk)
            v = torch.einsum('btd,dhe->bhte', h, Wv)
            scores = torch.einsum('bhqe,bhke->bhqk', q, k) * scale
            scores = torch.where(mask[None, None], scores, -1000000000.0)
            p = _softmax(scores, axis=-1)
            o = torch.einsum('bhqk,bhke->bhqe', p, v)
            o = torch.einsum('bhte,hed->btd', o, Wo)
            mem = o[:, :M, :].clone()
            outs.append(o[:, M:, :])
        return torch.concatenate(outs, axis=1)

    def _B_ref(x, w1, b1, w2, b2):
        h = torch.einsum('bsd,df->bsf', x, w1) + b1
        h = _gelu(h)
        return torch.einsum('bsf,fd->bsd', h, w2) + b2
    x = A_x
    h = x + _A_ref(_layernorm(x, axis=-1), A_mem0, A_Wq, A_Wk, A_Wv, A_Wo, A_seg_len)
    h2 = h + _B_ref(_layernorm(h, axis=-1), B_w1, B_b1, B_w2, B_b2)
    return h2
```

```cpp
#include <hip/hip_runtime.h>
#include <hip/hip_bf16.h>

typedef unsigned u2 __attribute__((ext_vector_type(2)));
typedef unsigned u4 __attribute__((ext_vector_type(4)));
typedef unsigned u8v __attribute__((ext_vector_type(8)));
typedef float f4 __attribute__((ext_vector_type(4)));
typedef __bf16 bf2 __attribute__((ext_vector_type(2)));
typedef __bf16 bf4 __attribute__((ext_vector_type(4)));
typedef __bf16 bf8 __attribute__((ext_vector_type(8)));
typedef float f2 __attribute__((ext_vector_type(2)));
#define LAS __attribute__((address_space(3)))
#define GAS __attribute__((address_space(1)))
#define DEV __device__ __forceinline__
#define RLX_AGENT __ATOMIC_RELAXED, __HIP_MEMORY_SCOPE_AGENT

#define DELAY1 2
#define DELAY2 2

constexpr int NSEG = 64;
constexpr unsigned SENT = 0xFFFFFFFFu;
constexpr unsigned MB = 1u << 20;
constexpr unsigned WS_PART0 = 0, WS_PART1 = 2 * MB, WS_MEM = 4 * MB;
constexpr unsigned WS_CTL = 6 * MB;
constexpr unsigned WS_XN = 7 * MB;
constexpr unsigned WS_H = 10 * MB, WS_HN = 13 * MB;
constexpr unsigned WS_QKV = 16 * MB;
constexpr unsigned WS_O = 64 * MB;
constexpr unsigned WS_H1 = 80 * MB;
constexpr unsigned WS_PO = 100 * MB, WS_PF = 120 * MB;
constexpr unsigned WS_END = 140 * MB;
constexpr int CL_ABORT = 0, CL_SEL = 1, CL_QKV = 8, CL_ATT = CL_QKV + 64, CL_O = CL_ATT + 128, CL_R1 = CL_O + 144,
              CL_F1 = CL_R1 + 32, CL_F2 = CL_F1 + 144, CL_CLAIM = CL_F2 + 144, CL_END = CL_CLAIM + 256;
constexpr unsigned OOB = 0x7fffffffu;
constexpr unsigned SPIN_CAP = 1u << 18;

struct Params {
    const unsigned short* x; const unsigned short* mem0; const unsigned short* wq; const unsigned short* wk;
    const unsigned short* wv; const unsigned short* wo; const unsigned short* w1; const unsigned short* b1;
    const unsigned short* w2; const unsigned short* b2; unsigned short* out; char* ws;
    unsigned rolemask; unsigned pad0;
};

typedef __amdgpu_buffer_rsrc_t rsrc_t;
DEV u4 ld16(rsrc_t r, unsigned vo, unsigned so) { return __builtin_amdgcn_raw_buffer_load_b128(r, vo, so, 16); }
DEV u2 ld8(rsrc_t r, unsigned vo, unsigned so) { return __builtin_amdgcn_raw_buffer_load_b64(r, vo, so, 16); }
DEV unsigned ld4(rsrc_t r, unsigned vo, unsigned so) { return __builtin_amdgcn_raw_buffer_load_b32(r, vo, so, 16); }
DEV void st16(rsrc_t r, u4 v, unsigned vo, unsigned so) {
    __builtin_amdgcn_raw_buffer_store_b128(v, r, vo, so, 16);
    asm volatile("s_nop 1" :: "v"(v) : "memory");
}
DEV void st8(rsrc_t r, u2 v, unsigned vo, unsigned so) { __builtin_amdgcn_raw_buffer_store_b64(v, r, vo, so, 16); }
DEV void st4(rsrc_t r, unsigned v, unsigned vo, unsigned so) { __builtin_amdgcn_raw_buffer_store_b32(v, r, vo, so, 16); }
#define CBAR() asm volatile("" ::: "memory")
#define VM_WAIT(n) asm volatile("s_waitcnt vmcnt(" #n ")" ::: "memory")
#define BARRIER() do { asm volatile("s_waitcnt lgkmcnt(0)" ::: "memory"); __builtin_amdgcn_s_barrier(); CBAR(); } while (0)
#define SCHED() __builtin_amdgcn_sched_barrier(0)

DEV unsigned pk_bf16(float lo, float hi) { const bf2 v = __builtin_convertvector((f2){lo, hi}, bf2); return __builtin_bit_cast(unsigned, v); }
DEV float bf_lo(unsigned u) { return __uint_as_float(u << 16); }
DEV float bf_hi(unsigned u) { return __uint_as_float(u & 0xffff0000u); }
DEV float rbf(float v) { return bf_lo(pk_bf16(v, v)); }
DEV unsigned xcc_id() { return (unsigned)__builtin_amdgcn_s_getreg(20 | (3 << 11)) & 7u; }
DEV void glds16(const void* gsrc, unsigned lds_dst) {
    unsigned keep;
    asm volatile("s_mov_b32 %0, m0\n\ts_mov_b32 m0, %2\n\ts_nop 0\n\tglobal_load_lds_dwordx4 %1, off\n\ts_mov_b32 m0, %0"
                 : "=&s"(keep) : "v"(gsrc), "s"(lds_dst) : "memory");
}
DEV u2 trd(LAS char* p) { return __builtin_bit_cast(u2, __builtin_amdgcn_ds_read_tr16_b64_v4bf16((LAS bf4*)p)); }
DEV u4 cat(u2 a, u2 b) { return (u4){a.x, a.y, b.x, b.y}; }
DEV u8v cat(u4 a, u4 b) { return __builtin_shufflevector(a, b, 0, 1, 2, 3, 4, 5, 6, 7); }
DEV f4 mfma32(u4 a, u4 b, f4 c) { return __builtin_amdgcn_mfma_f32_16x16x32_bf16(__builtin_bit_cast(bf8, a), __builtin_bit_cast(bf8, b), c, 0, 0, 0); }
DEV float wave_sum(float v) {
#pragma unroll
    for (int o = 1; o < 64; o <<= 1) v += __shfl_xor(v, o);
    return v;
}
DEV float swap_add(float a, float b) {
    const auto r = __builtin_amdgcn_permlane32_swap(__float_as_uint(a), __float_as_uint(b), false, false);
    return __uint_as_float(r[0]) + __uint_as_float(r[1]);
}
DEV float xg_max(float v) {
    auto r = __builtin_amdgcn_permlane16_swap(__float_as_uint(v), __float_as_uint(v), false, false);
    v = fmaxf(__uint_as_float(r[0]), __uint_as_float(r[1]));
    r = __builtin_amdgcn_permlane32_swap(__float_as_uint(v), __float_as_uint(v), false, false);
    return fmaxf(__uint_as_float(r[0]), __uint_as_float(r[1]));
}
DEV float xg_sum(float v) {
    auto r = __builtin_amdgcn_permlane16_swap(__float_as_uint(v), __float_as_uint(v), false, false);
    v = __uint_as_float(r[0]) + __uint_as_float(r[1]);
    r = __builtin_amdgcn_permlane32_swap(__float_as_uint(v), __float_as_uint(v), false, false);
    return __uint_as_float(r[0]) + __uint_as_float(r[1]);
}
DEV bool spin_out(unsigned& spins, rsrc_t rw) {
    ++spins;
    if ((spins & 1023u) != 0u) return false;
    if (spins >= SPIN_CAP) { st4(rw, 1u, WS_CTL + CL_ABORT * 128, 0); return true; }
    return __builtin_amdgcn_readfirstlane(ld4(rw, WS_CTL + CL_ABORT * 128, 0)) != 0u;
}
DEV unsigned wait_prog(rsrc_t rw, unsigned vo, bool act, unsigned need) {
    unsigned spins = 0;
    for (;;) {
        CBAR();
        unsigned v = ld4(rw, vo, 0);
        v = act ? v : 0xffffffffu;
        if (__all(v >= need)) {
            unsigned lo = need;
            if (__all(v >= need + 4u)) lo = need + 4u; else if (__all(v >= need + 1u)) lo = need + 1u;
            if (__all(v >= (unsigned)NSEG)) lo = NSEG;
            __builtin_amdgcn_fence(__ATOMIC_ACQUIRE, "wavefront");
            return lo;
        }
        if (spin_out(spins, rw)) return NSEG;
        __builtin_amdgcn_s_sleep(1);
    }
}
DEV void layernorm12(float (&v)[12], bool third) {
    float s = 0.f;
#pragma unroll
    for (int i = 0; i < 12; ++i) s += v[i];
    const float mean = wave_sum(s) * (1.0f / 576.0f);
    float q = 0.f;
#pragma unroll
    for (int i = 0; i < 12; ++i) { const float d = (i < 8 || third) ? v[i] - mean : 0.f; q += d * d; }
    const float var = wave_sum(q) * (1.0f / 576.0f);
    const float mb = rbf(mean), sd = rbf(sqrtf(rbf(rbf(var) + 1e-5f)));
#pragma unroll
    for (int i = 0; i < 12; ++i) v[i] = rbf(v[i] - mb) / sd;
}

__global__ void __launch_bounds__(256) k_init(Params p) {
    const rsrc_t rw = __builtin_amdgcn_make_buffer_rsrc(p.ws, 0, WS_END, 0x00020000);
    const int tid = threadIdx.x, lane = tid & 63, wave = tid >> 6, b = blockIdx.x;
#pragma unroll
    for (int rr = 0; rr < 2; ++rr) {
        const int row = b * 8 + wave * 2 + rr;
        const u2* src = (const u2*)(p.x + row * 576);
        const bool third = lane < 16;
        u2 r[3]; r[0] = src[lane]; r[1] = src[lane + 64]; r[2] = third ? src[lane + 128] : (u2){0u, 0u};
        float v[12];
#pragma unroll
        for (int i = 0; i < 3; ++i) { v[4 * i] = bf_lo(r[i].x); v[4 * i + 1] = bf_hi(r[i].x); v[4 * i + 2] = bf_lo(r[i].y); v[4 * i + 3] = bf_hi(r[i].y); }
        layernorm12(v, third);
#pragma unroll
        for (int i = 0; i < 3; ++i) {
            const int pi = lane + 64 * i;
            if (i < 2 || third) {
                char* dst = p.ws + WS_XN + (row >> 4) * 18432 + (pi >> 3) * 1024 + ((pi & 3) * 16 + (row & 15)) * 16 + ((pi >> 2) & 1) * 8;
                *(u2*)dst = (u2){pk_bf16(v[4 * i], v[4 * i + 1]), pk_bf16(v[4 * i + 2], v[4 * i + 3])};
            }
        }
    }
    const u4 sv = {SENT, SENT, SENT, SENT};
    const unsigned gt = b * 256 + tid;
    for (unsigned u = gt; u < 73728u; u += 65536u) { st16(rw, sv, WS_PART0 + u * 16, 0); st16(rw, sv, WS_PART1 + u * 16, 0); }
    for (unsigned u = gt; u < 63u * 1152u; u += 65536u) {
        const unsigned s = u / 1152u + 1u, c = u % 1152u;
        const unsigned off = c < 1024u ? c * 16 : 16384u + ((c - 1024u) >> 6) * 8192u + (c & 63u) * 16;
        st16(rw, sv, WS_MEM + s * 32768u + off, 0);
    }
    if (gt < (unsigned)CL_END) st4(rw, 0u, WS_CTL + gt * 128, 0);
    if (b == 255) {
        for (int pc = tid; pc < 576; pc += 256) {
            const int L = pc >> 3, u = (pc >> 2) & 1, i = pc & 3, cb = L >> 1, g = L & 1;
            const unsigned short* src = p.mem0 + (4 * g + i) * 576 + 16 * cb + 8 * u;
            u4 v;
            v.x = src[0] | ((unsigned)src[2] << 16); v.y = src[1] | ((unsigned)src[3] << 16);
            v.z = src[4] | ((unsigned)src[6] << 16); v.w = src[5] | ((unsigned)src[7] << 16);
            v.x = min(v.x, SENT - 1); v.y = min(v.y, SENT - 1); v.z = min(v.z, SENT - 1); v.w = min(v.w, SENT - 1);
            const unsigned off = pc * 16, ph = ((off >> 13) << 14) | (off & 8191u);
            st16(rw, v, WS_MEM + ph, 0); st16(rw, v, WS_MEM + ph + 8192u, 0);
        }
    }
}

#define SMF(acc, a, b, idx, bc) asm volatile("v_smfmac_f32_16x16x64_bf16 %0, %1, %2, %3" : "+v"(acc) : "v"(a), bc(b), "v"(idx))
#define NOP2() asm volatile("s_nop 1")

template <int I> DEV void smf_w(f4& acc, const u4& a, const u8v& w, unsigned idx) {
    if constexpr (I < 32) SMF(acc, a, w, idx, "a"); else SMF(acc, a, w, idx, "v");
}
DEV void mf32(f4& d, const u4& a, const u4& b) { asm volatile("v_mfma_f32_16x16x32_bf16 %0, %1, %2, %0" : "+v"(d) : "v"(a), "v"(b)); }
DEV void mf32z(f4& d, const u4& a, const u4& b) { asm volatile("v_mfma_f32_16x16x32_bf16 %0, %1, %2, 0" : "=&v"(d) : "v"(a), "v"(b)); }
DEV void mf32c(f4& d, const u4& a, const u4& b, const f4& c) { asm volatile("v_mfma_f32_16x16x32_bf16 %0, %1, %2, %3" : "=&v"(d) : "v"(a), "v"(b), "v"(c)); }
DEV void mf16(f4& d, const u2& a, const u2& b) { asm volatile("v_mfma_f32_16x16x16_bf16 %0, %1, %2, %0" : "+v"(d) : "v"(a), "v"(b)); }

template <int N, class Fn> DEV void sfor(Fn&& f) { if constexpr (N > 0) { sfor<N - 1>(f); f(std::integral_constant<int, N - 1>{}); } }

struct SoftOut { u4 p01; u2 p2; float l; };
DEV SoftOut soft12(const f4& s0, const f4& s1, const f4& s2, float c) {
    float m = fmaxf(fmaxf(fmaxf(s0[0], s0[1]), fmaxf(s0[2], s0[3])), fmaxf(fmaxf(s1[0], s1[1]), fmaxf(s1[2], s1[3])));
    m = fmaxf(m, fmaxf(fmaxf(s2[0], s2[1]), fmaxf(s2[2], s2[3])));
    m = xg_max(m);
    const float mc = m * c;
    float e[12];
#pragma unroll
    for (int i = 0; i < 4; ++i) {
        e[i] = __builtin_amdgcn_exp2f(__builtin_fmaf(s0[i], c, -mc));
        e[4 + i] = __builtin_amdgcn_exp2f(__builtin_fmaf(s1[i], c, -mc));
        e[8 + i] = __builtin_amdgcn_exp2f(__builtin_fmaf(s2[i], c, -mc));
    }
    SoftOut o;
    o.p01 = (u4){pk_bf16(e[0], e[1]), pk_bf16(e[2], e[3]), pk_bf16(e[4], e[5]), pk_bf16(e[6], e[7])};
    o.p2 = (u2){pk_bf16(e[8], e[9]), pk_bf16(e[10], e[11])};
    o.l = ((e[0] + e[1]) + (e[2] + e[3])) + ((e[4] + e[5]) + (e[6] + e[7])) + ((e[8] + e[9]) + (e[10] + e[11]));
    return o;
}

constexpr int C_WO = 0, C_QK = 73728, C_VM = C_QK + 4352, C_KX = C_VM + 1024, C_WV = C_KX + 40960;

DEV void role_chain(const Params& p, LAS char* L, const int c, const rsrc_t rw) {
    const int lane = threadIdx.x & 63, wave = __builtin_amdgcn_readfirstlane(threadIdx.x >> 6);
    const int h = c >> 1, half = c & 1;
    const unsigned lbase = (unsigned)(size_t)L;
    const int G = lane >> 4, q4 = (lane >> 2) & 3, pp = lane & 3, n16 = lane & 15;
    u8v W[45];
    {
        const char* gq = (const char*)p.wq + h * 256, * gk = (const char*)p.wk + h * 256, * gv = (const char*)p.wv + h * 256 + half * 128;
        auto issue = [&](int ks) {
            const unsigned slot = lbase + (ks & 3) * 40960;
#pragma unroll
            for (int i = 0; i < 4; ++i) {
                const int d = wave * 16 + i * 4 + (lane >> 4);
                const size_t off = (size_t)(64 * ks + d) * 8192 + (((lane & 15) ^ (d & 14)) * 16);
                glds16(gq + off, slot + (wave * 4 + i) * 1024);
                glds16(gk + off, slot + 16384 + (wave * 4 + i) * 1024);
            }
#pragma unroll
            for (int i = 0; i < 2; ++i) {
                const int rho = wave * 16 + i * 8 + (lane >> 3);
                const int d = (rho & ~3) | ((rho & 1) << 1) | ((rho >> 1) & 1);
                const size_t off = (size_t)(64 * ks + d) * 8192 + (((lane & 7) ^ (((d >> 2) & 3) * 2)) * 16);
                glds16(gv + off, slot + 32768 + (wave * 2 + i) * 1024);
            }
        };
        auto issue_wo = [&](int part) {
            const char* go = (const char*)p.wo + (size_t)(h * 128 + half * 64) * 1152;
#pragma unroll
            for (int i = 0; i < 10; ++i) {
                const int ins = part * 36 + wave * 9 + (i < 9 ? i : 8);
                const int u = ins * 64 + lane, e = u / 72, cc = u % 72;
                glds16(go + (size_t)e * 1152 + ((cc ^ (((e >> 4) & 1) * 4)) * 16), lbase + 81920 + ins * 1024);
            }
        };
        issue(0); issue(1); issue(2); issue(3);
        const int p_ = q4 >> 1, b_ = q4 & 1;
        const int dl = 16 * (G >> 1) + 8 * p_ + 4 * (G & 1) + 2 * b_;
        const int aq0 = dl * 256 + (((4 * wave + (pp >> 1)) ^ (dl & 14)) * 16) + (pp & 1) * 8, aq1 = aq0 ^ 32;
        const int rl = 16 * (G >> 1) + 8 * p_ + 4 * (G & 1) + b_;
        const int av = 32768 + rl * 128 + (((2 * wave + (pp >> 1)) ^ (((dl >> 2) & 3) * 2)) * 16) + (pp & 1) * 8;
        sfor<9>([&](auto KS) {
            constexpr int ks = KS;
            if constexpr (ks <= 5) VM_WAIT(30); else if constexpr (ks == 6) VM_WAIT(20); else if constexpr (ks == 7) VM_WAIT(20); else VM_WAIT(20);
            BARRIER();
            LAS char* S = L + (ks & 3) * 40960;
#pragma unroll
            for (int t = 0; t < 4; ++t) {
                LAS char* a = S + (t >> 1) * 16384 + ((t & 1) ? aq1 : aq0);
                W[ks * 4 + t] = cat(cat(trd(a), trd(a + 256)), cat(trd(a + 8192), trd(a + 8192 + 256)));
            }
            { LAS char* a = S + av; W[36 + ks] = cat(cat(trd(a), trd(a + 256)), cat(trd(a + 4096), trd(a + 4096 + 256))); }
            BARRIER();
            if constexpr (ks + 4 < 9) issue(ks + 4); else if constexpr (ks == 6) issue_wo(0); else if constexpr (ks == 7) issue_wo(1);
        });
        VM_WAIT(0);
        BARRIER();
        const int el = 16 * (G & 1) + 4 * (G >> 1) + b_;
#pragma unroll 1
        for (int f = wave; f < 36; f += 4) {
            const int dt = f >> 1, ks = f & 1;
            LAS char* a = L + 81920 + (32 * ks + el) * 1152 + (((4 * dt + 2 * p_ + (pp >> 1)) ^ ((G & 1) * 4)) * 16) + (pp & 1) * 8;
            const u4 lo = cat(trd(a), trd(a + 2 * 1152)), hi = cat(trd(a + 8 * 1152), trd(a + 10 * 1152));
            *(LAS u4*)(L + C_WO + f * 2048 + lane * 16) = lo; *(LAS u4*)(L + C_WO + f * 2048 + 1024 + lane * 16) = hi;
        }
        BARRIER();
#pragma unroll
        for (int i = 0; i < 5; ++i) {
            *(LAS u4*)(L + C_WV + wave * 10240 + i * 2048 + lane * 16) = __builtin_shufflevector(W[40 + i], W[40 + i], 0, 1, 2, 3);
            *(LAS u4*)(L + C_WV + wave * 10240 + i * 2048 + 1024 + lane * 16) = __builtin_shufflevector(W[40 + i], W[40 + i], 4, 5, 6, 7);
        }
    }
    unsigned sel;
    { unsigned spins = 0;
      for (;;) { CBAR(); sel = __builtin_amdgcn_readfirstlane(ld4(rw, WS_CTL + CL_SEL * 128, 0)); if (sel & 8u) break; if (spin_out(spins, rw)) break; __builtin_amdgcn_s_sleep(1); } }
    const unsigned sb0 = WS_PART0 + (sel & 1u) * 8192u, sb1 = WS_PART1 + ((sel >> 1) & 1u) * 8192u, sbm = WS_MEM + ((sel >> 2) & 1u) * 8192u;
    const unsigned idx = (lane & 8) ? 0xEEEEu : 0x4444u;
    const unsigned l16 = lane * 16;
    const unsigned vmem = (G * 2 + ((lane >> 2) & 1)) * 128 + (((lane >> 3) & 1) * 4 + (lane & 3)) * 16;
    const int ndt = wave >= 2 ? 5 : 4, dt0 = wave >= 2 ? 8 + (wave - 2) * 5 : wave * 4;
    const unsigned vpart = G * 16384 + ((c ^ G) & 3) * 128 + n16 * 8;
    unsigned spart[5];
#pragma unroll
    for (int j = 0; j < 5; ++j) spart[j] = j < ndt ? (dt0 + j) * 65536 + (((c >> 2) + dt0 + j) & 15) * 512 : 0x40000000u;
    const int task = wave == 0 ? c : (wave == 1 && (c & 7) == 0) ? 64 + (c >> 3) : -1;
    const unsigned sred = task * 16384;
    const float scl = 0.08838834764831845f, l2e = 1.4426950408889634f;
    const unsigned skx = WS_QKV + (256 + h * 8 + 2 * wave) * 1024, svx = WS_QKV + (512 + h * 8 + half * 4 + wave) * 1024, sqx = WS_QKV + (h * 8 + 2 * wave) * 1024;
    const unsigned so_ = WS_O + ((wave & 1) * 128 + c * 2) * 1024;
    LAS char* aqk_w = L + C_QK + (G & 1) * 2176 + (32 * wave + 16 * (lane >> 5) + n16) * 16;
    LAS char* aqk_r = L + C_QK + (pp & 1) * 2176 + (4 * G + q4) * 16;
    LAS char* avm_w = L + C_VM + (16 * wave + n16) * 16 + (G & 1) * 8 + (lane >> 5) * 4;
    LAS char* avm_r = L + C_VM + n16 * 16 + (G & 1) * 8;
    const int vx_r = (pp * 16 + ((4 * G + q4) ^ ((pp & 1) * 8))) * 16;
    unsigned known = 0;
    auto opaque_lane = [&]() { int l_ = lane; asm volatile("" : "+v"(l_)); return l_; };
    auto mk_cpad = [&]() { const float v = opaque_lane() >= 32 ? -__builtin_inff() : 0.f; return (f4){v, v, v, v}; };
    auto prog_wait = [&](unsigned need) {
        const int l_ = opaque_lane(), col = (l_ >> 1) * 4096 + h * 128 + (l_ & 1) * 127;
        return wait_prog(rw, l_ < 6 ? WS_CTL + (CL_QKV + col / 192) * 128 : OOB, l_ < 6, need);
    };
    auto stage = [&](int buf, const u4 (&pf)[5]) {
        LAS char* B = L + C_KX + buf * 20480 + wave * 1024;
        *(LAS u4*)(B + l16) = (u4){pf[0].x, pf[0].y, pf[1].x, pf[1].y};
        *(LAS u4*)(B + 4096 + l16) = (u4){pf[0].z, pf[0].w, pf[1].z, pf[1].w};
        *(LAS u4*)(B + 8192 + l16) = (u4){pf[2].x, pf[2].y, pf[3].x, pf[3].y};
        *(LAS u4*)(B + 12288 + l16) = (u4){pf[2].z, pf[2].w, pf[3].z, pf[3].w};
        *(LAS u4*)(B + 16384 + (l16 ^ ((l16 >> 1) & 128))) = pf[4];
    };
    auto fetch = [&](int sg, u4 (&pf)[5]) {
        const unsigned sn = (unsigned)min(sg, NSEG - 1) * 786432u;
        pf[0] = ld16(rw, l16, sn + skx); pf[1] = ld16(rw, l16 + 1024, sn + skx);
        pf[2] = ld16(rw, l16, sn + sqx); pf[3] = ld16(rw, l16 + 1024, sn + sqx); pf[4] = ld16(rw, l16, sn + svx);
    };
    known = prog_wait(2);
    { u4 pf[5]; fetch(0, pf); stage(0, pf); }

#pragma unroll 1
    for (int s = 0; s < NSEG; ++s) {
        const int buf = s & 1;
        const unsigned sb = buf ? sb1 : sb0, sbp = buf ? sb0 : sb1;
        LAS char* XB = L + C_KX + buf * 20480;
        BARRIER();
        if (DELAY2 > 0) __builtin_amdgcn_s_sleep(DELAY2);
        const unsigned som = sbm + s * 32768u;
        u4 a[9];
#pragma unroll
        for (int ks = 0; ks < 9; ++ks) a[ks] = ld16(rw, vmem + (ks & 3) * 1024, som + (ks < 8 ? (ks >> 2) * 4096 : 16384));
        u4 kx[8];
#pragma unroll
        for (int i = 0; i < 8; ++i) kx[i] = *(LAS u4*)(XB + i * 1024 + l16);
        u8v wv[5];
#pragma unroll
        for (int i = 0; i < 5; ++i) { LAS char* a_ = L + C_WV + wave * 10240 + i * 2048 + l16; wv[i] = cat(*(LAS u4*)a_, *(LAS u4*)(a_ + 1024)); }
        f4 acc[5];
#pragma unroll
        for (int t = 0; t < 5; ++t) acc[t] = (f4){0.f, 0.f, 0.f, 0.f};
        f4 cpad = mk_cpad();
        SCHED();
        sfor<9>([&](auto KS) {
            constexpr int ks = KS;
            if (__builtin_expect(__any(max(max(a[ks].x, a[ks].y), max(a[ks].z, a[ks].w)) == SENT), 0)) {
                unsigned spins = 0;
                do { CBAR(); a[ks] = ld16(rw, vmem + (ks & 3) * 1024, som + (ks < 8 ? (ks >> 2) * 4096 : 16384));
                     if (spin_out(spins, rw)) break;
                } while (__any(max(max(a[ks].x, a[ks].y), max(a[ks].z, a[ks].w)) == SENT));
            }
            NOP2();
            smf_w<ks * 4 + 0>(acc[0], a[ks], W[ks * 4 + 0], idx); smf_w<ks * 4 + 1>(acc[1], a[ks], W[ks * 4 + 1], idx);
            smf_w<ks * 4 + 2>(acc[2], a[ks], W[ks * 4 + 2], idx); smf_w<ks * 4 + 3>(acc[3], a[ks], W[ks * 4 + 3], idx);
        });
        sfor<2>([&](auto KS) { smf_w<36 + KS>(acc[4], a[KS], W[36 + KS], idx); });
        asm volatile("s_nop 5" : "+v"(acc[0]), "+v"(acc[1]), "+v"(acc[2]), "+v"(acc[3]));
        {
            float qs[4], ks_[4];
#pragma unroll
            for (int i = 0; i < 4; ++i) { qs[i] = swap_add(acc[0][i], acc[1][i]); ks_[i] = swap_add(acc[2][i], acc[3][i]); }
            *(LAS u4*)aqk_w = (u4){pk_bf16(qs[0], qs[1]), pk_bf16(qs[2], qs[3]), pk_bf16(ks_[0], ks_[1]), pk_bf16(ks_[2], ks_[3])};
        }
        SCHED();
        sfor<2>([&](auto KS) { smf_w<38 + KS>(acc[4], a[2 + KS], W[38 + KS], idx); });
        sfor<5>([&](auto KS) { smf_w<40 + KS>(acc[4], a[4 + KS], wv[KS], idx); });
        BARRIER();
        u4 pf[5];
        f4 s0, s1, s2;
        {
            u4 qf[4], kf[4];
#pragma unroll
            for (int ks = 0; ks < 4; ++ks) qf[ks] = cat(trd(aqk_r + ks * 512), trd(aqk_r + ks * 512 + 256));
#pragma unroll
            for (int ks = 0; ks < 4; ++ks) kf[ks] = cat(trd(aqk_r + ks * 512 + 8), trd(aqk_r + ks * 512 + 256 + 8));
            VM_WAIT(0);
            if (wave >= 2 && s > 0 && lane == 0) st4(rw, (unsigned)s, WS_CTL + (CL_ATT + c * 2 + (wave & 1)) * 128, 0);
            fetch(s + 1, pf);
            if (task >= 0 && s > 0) {
                const u4 sv = {SENT, SENT, SENT, SENT};
#pragma unroll
                for (int i = 0; i < 8; ++i) st16(rw, sv, l16 + (i & 3) * 1024, sbp + sred + (i >> 2) * 4096);
            }
            asm volatile("s_nop 7" : "+v"(acc[4]));
            *(LAS unsigned*)avm_w = pk_bf16(swap_add(acc[4][0], acc[4][2]), swap_add(acc[4][1], acc[4][3]));
            NOP2();
            mf32z(s0, kx[0], qf[0]); mf32z(s1, kx[4], qf[0]);
#pragma unroll
            for (int ks = 1; ks < 4; ++ks) { mf32(s0, kx[ks], qf[ks]); mf32(s1, kx[4 + ks], qf[ks]); }
            mf32c(s2, kf[0], qf[0], cpad);
#pragma unroll
            for (int ks = 1; ks < 4; ++ks) mf32(s2, kf[ks], qf[ks]);
        }
        BARRIER();
        if (s < NSEG - 1) {
            u4 vx[4]; u2 vm[4];
#pragma unroll
            for (int t = 0; t < 4; ++t) { LAS char* a_ = XB + 16384 + t * 1024 + vx_r; vx[t] = cat(trd(a_), trd(a_ + 8)); }
#pragma unroll
            for (int t = 0; t < 4; ++t) vm[t] = *(LAS u2*)(avm_r + t * 256);
            LAS char* WB = L + C_WO + dt0 * 4096 + l16;
            u8v wf[10];
#pragma unroll
            for (int j = 0; j < 6; ++j) wf[j] = cat(*(LAS u4*)(WB + j * 2048), *(LAS u4*)(WB + j * 2048 + 1024));
            asm volatile("s_nop 7" : "+v"(s0), "+v"(s1), "+v"(s2));
            SoftOut sm = soft12(s0, s1, s2, scl * l2e);
            f4 o[4];
            asm volatile("s_nop 1" : "+v"(sm.p01), "+v"(sm.p2));
#pragma unroll
            for (int t = 0; t < 4; ++t) mf32z(o[t], vx[t], sm.p01);
#pragma unroll
            for (int t = 0; t < 4; ++t) mf16(o[t], vm[t], sm.p2);
            CBAR();
#pragma unroll
            for (int j = 6; j < 10; ++j) wf[j] = cat(*(LAS u4*)(WB + j * 2048), *(LAS u4*)(WB + j * 2048 + 1024));
            const float inv = __builtin_amdgcn_rcpf(xg_sum(sm.l));
            f4 po[5];
#pragma unroll
            for (int j = 0; j < 5; ++j) po[j] = (f4){0.f, 0.f, 0.f, 0.f};
            asm volatile("s_nop 7" : "+v"(o[0]), "+v"(o[1]), "+v"(o[2]), "+v"(o[3]));
            u4 of[2];
#pragma unroll
            for (int ks = 0; ks < 2; ++ks)
                of[ks] = (u4){pk_bf16(o[2 * ks][0] * inv, o[2 * ks][1] * inv), pk_bf16(o[2 * ks][2] * inv, o[2 * ks][3] * inv),
                              pk_bf16(o[2 * ks + 1][0] * inv, o[2 * ks + 1][1] * inv), pk_bf16(o[2 * ks + 1][2] * inv, o[2 * ks + 1][3] * inv)};
            asm volatile("s_nop 1" : "+v"(of[0]), "+v"(of[1]), "+v"(po[0]), "+v"(po[1]), "+v"(po[2]), "+v"(po[3]), "+v"(po[4]));
#pragma unroll
            for (int j = 0; j < 5; ++j) { SMF(po[j], of[0], wf[2 * j], idx, "v"); SMF(po[j], of[1], wf[2 * j + 1], idx, "v"); }
            asm volatile("s_nop 7" : "+v"(po[0]), "+v"(po[1]), "+v"(po[2]), "+v"(po[3]), "+v"(po[4]));
#pragma unroll
            for (int j = 0; j < 5; ++j) st8(rw, (u2){pk_bf16(po[j][0], po[j][1]), pk_bf16(po[j][2], po[j][3])}, vpart, sb + spart[j]);
            SCHED();
            if (task >= 0) {
                const int l_ = opaque_lane();
                const unsigned one = (l_ & 1) ? 0x3f800000u : 0x00003f80u; const int w_ = (l_ & 15) >> 1;
                u4 bsel = (u4){w_ == 0 ? one : 0u, w_ == 1 ? one : 0u, w_ == 2 ? one : 0u, w_ == 3 ? one : 0u};
                const unsigned vout = (l_ & 8) ? OOB : (((l_ >> 4) & 1) * 4 + (l_ & 3)) * 16 + ((l_ >> 5) * 2 + ((l_ >> 2) & 1)) * 4;
                if (DELAY1 > 0) __builtin_amdgcn_s_sleep(DELAY1);
                unsigned spins = 0;
                float u0, u1;
                for (;;) {
                    CBAR();
                    u4 r[8];
#pragma unroll
                    for (int i = 0; i < 8; ++i) r[i] = ld16(rw, l16 + (i & 3) * 1024, sb + sred + (i >> 2) * 4096);
                    f4 d;
                    asm volatile("s_nop 1" : "+v"(bsel));
                    mf32z(d, r[0], bsel);
#pragma unroll
                    for (int i = 1; i < 8; ++i) mf32(d, r[i], bsel);
                    asm volatile("s_nop 7" : "+v"(d));
                    u0 = swap_add(d[0], d[2]); u1 = swap_add(d[1], d[3]);
                    if (__builtin_expect(__all((u0 + u1) == (u0 + u1)), 1)) break;
                    unsigned mx = 0;
#pragma unroll
                    for (int i = 0; i < 8; ++i) mx = max(mx, max(max(r[i].x, r[i].y), max(r[i].z, r[i].w)));
                    if (!__any(mx == SENT)) break;
                    if (spin_out(spins, rw)) break;
                }
                st4(rw, min(pk_bf16(u0, u1), SENT - 1), vout, som + 32768u + (task >> 6) * 16384 + (task & 63) * 128);
            }
        }
        CBAR();
        stage(buf ^ 1, pf);
        if (wave >= 2) {
            const int rt = wave & 1;
            f4 x0, x1, x2;
            u4 qq[4], k2[8], kf[4], vx[4]; u2 vm[4];
#pragma unroll
            for (int ks = 0; ks < 4; ++ks) qq[ks] = *(LAS u4*)(XB + 8192 + rt * 4096 + ks * 1024 + l16);
#pragma unroll
            for (int i = 0; i < 8; ++i) k2[i] = *(LAS u4*)(XB + i * 1024 + l16);
#pragma unroll
            for (int ks = 0; ks < 4; ++ks) kf[ks] = cat(trd(aqk_r + ks * 512 + 8), trd(aqk_r + ks * 512 + 256 + 8));
#pragma unroll
            for (int t = 0; t < 4; ++t) { LAS char* a_ = XB + 16384 + t * 1024 + vx_r; vx[t] = cat(trd(a_), trd(a_ + 8)); }
#pragma unroll
            for (int t = 0; t < 4; ++t) vm[t] = *(LAS u2*)(avm_r + t * 256);
            f4 cp2 = mk_cpad();
            asm volatile("s_nop 1" : "+v"(cp2));
            mf32z(x0, k2[0], qq[0]); mf32z(x1, k2[4], qq[0]); mf32c(x2, kf[0], qq[0], cp2);
#pragma unroll
            for (int ks = 1; ks < 4; ++ks) { mf32(x0, k2[ks], qq[ks]); mf32(x1, k2[4 + ks], qq[ks]); mf32(x2, kf[ks], qq[ks]); }
            asm volatile("s_nop 7" : "+v"(x0), "+v"(x1), "+v"(x2));
            const int l_ = opaque_lane(), dd = rt * 16 + (l_ & 15) - 4 * (l_ >> 4);
#pragma unroll
            for (int i = 0; i < 4; ++i) {
                x0[i] = (i <= dd) ? x0[i] * scl : -1e9f;
                x1[i] = (16 + i <= dd) ? x1[i] * scl : -1e9f;
                x2[i] *= scl;
            }
            SoftOut sm = soft12(x0, x1, x2, l2e);
            f4 o[4];
            asm volatile("s_nop 1" : "+v"(sm.p01), "+v"(sm.p2));
#pragma unroll
            for (int t = 0; t < 4; ++t) mf32z(o[t], vx[t], sm.p01);
#pragma unroll
            for (int t = 0; t < 4; ++t) mf16(o[t], vm[t], sm.p2);
            const float inv = __builtin_amdgcn_rcpf(xg_sum(sm.l));
            asm volatile("s_nop 7" : "+v"(o[0]), "+v"(o[1]), "+v"(o[2]), "+v"(o[3]));
#pragma unroll
            for (int ks = 0; ks < 2; ++ks)
                st16(rw, (u4){pk_bf16(o[2 * ks][0] * inv, o[2 * ks][1] * inv), pk_bf16(o[2 * ks][2] * inv, o[2 * ks][3] * inv),
                              pk_bf16(o[2 * ks + 1][0] * inv, o[2 * ks + 1][1] * inv), pk_bf16(o[2 * ks + 1][2] * inv, o[2 * ks + 1][3] * inv)},
                     l16 + ks * 1024, so_ + s * 262144u);
        } else if (wave == 1) {
            const unsigned need = (unsigned)min(s + 3, NSEG);
            if (known < need) known = prog_wait(need);
        }
    }
    if (wave >= 2) { VM_WAIT(0); if (lane == 0) st4(rw, (unsigned)NSEG, WS_CTL + (CL_ATT + c * 2 + (wave & 1)) * 128, 0); }
}

DEV void role_qkv(const Params& p, LAS char* L, const int P, const rsrc_t rw) {
    const int lane = threadIdx.x & 63, wave = __builtin_amdgcn_readfirstlane(threadIdx.x >> 6);
    const unsigned lbase = (unsigned)(size_t)L;
    const int G = lane >> 4, q4 = (lane >> 2) & 3, pp = lane & 3;
    u4 W[54];
    const char* xn = p.ws + WS_XN;
    auto slot_off = [&](int s) { const int j = s & 3; return j == 0 ? 122880 : (j - 1) * 36864; };
    auto issue_x = [&](int s) {
        const char* src = xn + (size_t)min(s, NSEG - 1) * 36864 + wave * 9216 + lane * 16;
#pragma unroll
        for (int i = 0; i < 9; ++i) glds16(src + i * 1024, lbase + slot_off(s) + wave * 9216 + i * 1024);
    };
    {
        const char* src[6];
#pragma unroll
        for (int i = 0; i < 6; ++i) {
            const int u = (wave * 6 + i) * 64 + lane, r = u / 24, cc = u % 24;
            const int col = P * 192 + ((cc ^ (((r >> 1) & 3) * 2)) * 8), m_ = col >> 12;
            const unsigned short* w = m_ == 0 ? p.wq : m_ == 1 ? p.wk : p.wv;
            src[i] = (const char*)(w + (size_t)r * 4096 + (col & 4095));
        }
        auto issue = [&](int un) {
#pragma unroll
            for (int i = 0; i < 6; ++i) glds16(src[i] + (size_t)un * 64 * 8192, lbase + (un % 5) * 24576 + (wave * 6 + i) * 1024);
        };
        issue(0); issue(1); issue(2); issue(3); issue(4);
        const int rl = 4 * G + q4;
        int ar[3];
#pragma unroll
        for (int t = 0; t < 3; ++t) ar[t] = rl * 384 + (((6 * wave + 2 * t + (pp >> 1)) ^ (((rl >> 1) & 3) * 2)) * 16) + (pp & 1) * 8;
        sfor<9>([&](auto UN) {
            constexpr int un = UN;
            if constexpr (un <= 4) VM_WAIT(24); else if constexpr (un == 5) VM_WAIT(27); else if constexpr (un == 6) VM_WAIT(21);
            else if constexpr (un == 7) VM_WAIT(15); else VM_WAIT(9);
            BARRIER();
            LAS char* S = L + (un % 5) * 24576;
#pragma unroll
            for (int k2 = 0; k2 < 2; ++k2)
#pragma unroll
                for (int t = 0; t < 3; ++t) { LAS char* a = S + k2 * 32 * 384 + ar[t]; W[(un * 2 + k2) * 3 + t] = cat(trd(a), trd(a + 16 * 384)); }
            BARRIER();
            if constexpr (un + 5 < 9) issue(un + 5); else if constexpr (un == 4) issue_x(0);
        });
    }
    issue_x(1); issue_x(2);
    const unsigned vst = WS_QKV + (P * 12 + wave * 3) * 1024 + lane * 16;
#pragma unroll 1
    for (int s = 0; s < NSEG; ++s) {
        if (s == 0) VM_WAIT(18); else if (s <= 2) VM_WAIT(9); else VM_WAIT(21);
        BARRIER();
        if (s > 0 && threadIdx.x == 0) st4(rw, (unsigned)(s <= 2 ? s : s - 1), WS_CTL + (CL_QKV + P) * 128, 0);
        LAS char* S = L + slot_off(s) + lane * 16;
        f4 acc[2][3];
#pragma unroll
        for (int t = 0; t < 3; ++t) { acc[0][t] = (f4){0.f, 0.f, 0.f, 0.f}; acc[1][t] = acc[0][t]; }
#pragma unroll
        for (int ks = 0; ks < 18; ++ks) {
            const u4 x0 = *(LAS u4*)(S + ks * 1024), x1 = *(LAS u4*)(S + 18432 + ks * 1024);
#pragma unroll
            for (int t = 0; t < 3; ++t) { acc[0][t] = mfma32(W[ks * 3 + t], x0, acc[0][t]); acc[1][t] = mfma32(W[ks * 3 + t], x1, acc[1][t]); }
        }
#pragma unroll
        for (int t = 0; t < 3; ++t)
            st16(rw, (u4){pk_bf16(acc[0][t][0], acc[0][t][1]), pk_bf16(acc[0][t][2], acc[0][t][3]), pk_bf16(acc[1][t][0], acc[1][t][1]), pk_bf16(acc[1][t][2], acc[1][t][3])},
                 vst + t * 1024, s * 786432u);
        issue_x(s + 3);
    }
    VM_WAIT(0);
    BARRIER();
    if (threadIdx.x == 0) st4(rw, (unsigned)NSEG, WS_CTL + (CL_QKV + P) * 128, 0);
}

template <int KSW>
DEV void role_proj(LAS char* L, const unsigned short* wgt, const int kq, const int nj, const rsrc_t rw,
                   const unsigned ws_in, const unsigned ws_out, const unsigned vdep, const bool dep_act, const int cl_out) {
    const int lane = threadIdx.x & 63, wave = __builtin_amdgcn_readfirstlane(threadIdx.x >> 6);
    const unsigned lbase = (unsigned)(size_t)L;
    const int G = lane >> 4, q4 = (lane >> 2) & 3, pp = lane & 3, n16 = lane & 15;
    const int ks0 = (kq * 4 + wave) * KSW;
    u4 W[KSW * 4];
    __builtin_amdgcn_s_sleep(127);
    {
        const int r0 = lane >> 3;
        const char* src = (const char*)wgt + (size_t)(ks0 * 32 + r0) * 1152 + nj * 128 + (((lane & 7) ^ (((r0 >> 1) & 3) * 2)) * 16);
#pragma unroll
        for (int i = 0; i < KSW * 4; ++i) glds16(src + (size_t)i * 8 * 1152, lbase + wave * 40960 + i * 1024);
        VM_WAIT(0);
        const int rl = 4 * G + q4;
#pragma unroll
        for (int ks = 0; ks < KSW; ++ks)
#pragma unroll
            for (int t = 0; t < 4; ++t) {
                LAS char* a = L + wave * 40960 + ks * 4096 + rl * 128 + (((2 * t + (pp >> 1)) ^ (((rl >> 1) & 3) * 2)) * 16) + (pp & 1) * 8;
                W[ks * 4 + t] = cat(trd(a), trd(a + 2048));
            }
        BARRIER();
    }
    const unsigned vin = ws_in + ks0 * 1024 + lane * 16;
    const int c8 = lane & 7, rt_r = wave >> 1, nt_r = 2 * (wave & 1) + (c8 >> 2), g_r = c8 & 3;
    unsigned known = 0;
#pragma unroll 1
    for (int s = 0; s < NSEG; ++s) {
        if (known < (unsigned)s + 1u) known = wait_prog(rw, vdep, dep_act, s + 1);
        u4 a[2][KSW];
        const unsigned so = s * (unsigned)(2 * 16 * KSW * 1024);
#pragma unroll
        for (int ks = 0; ks < KSW; ++ks) { a[0][ks] = ld16(rw, vin + (ks & 3) * 1024, so + (ks >> 2) * 4096); a[1][ks] = ld16(rw, vin + (ks & 3) * 1024, so + 16 * KSW * 1024 + (ks >> 2) * 4096); }
        SCHED();
        f4 acc[2][4];
#pragma unroll
        for (int t = 0; t < 4; ++t) { acc[0][t] = (f4){0.f, 0.f, 0.f, 0.f}; acc[1][t] = acc[0][t]; }
#pragma unroll
        for (int ks = 0; ks < KSW; ++ks)
#pragma unroll
            for (int t = 0; t < 4; ++t) { acc[0][t] = mfma32(W[ks * 4 + t], a[0][ks], acc[0][t]); acc[1][t] = mfma32(W[ks * 4 + t], a[1][ks], acc[1][t]); }
        LAS char* R = L + (s & 1) * 32768;
#pragma unroll
        for (int rt = 0; rt < 2; ++rt)
#pragma unroll
            for (int t = 0; t < 4; ++t) *(LAS f4*)(R + wave * 8192 + (rt * 4 + t) * 1024 + G * 256 + ((n16 + 2 * (G + 4 * (t & 1))) & 15) * 16) = acc[rt][t];
        BARRIER();
#pragma unroll
        for (int hf = 0; hf < 2; ++hf) {
            const int row = (lane >> 3) + 8 * hf;
            LAS char* a_ = R + (rt_r * 4 + nt_r) * 1024 + g_r * 256 + ((row + 2 * c8) & 15) * 16;
            const f4 v = (*(LAS f4*)a_ + *(LAS f4*)(a_ + 8192)) + (*(LAS f4*)(a_ + 16384) + *(LAS f4*)(a_ + 24576));
            st16(rw, __builtin_bit_cast(u4, v), ws_out + (kq * 32 + rt_r * 16 + row) * 2304 + nj * 256 + (wave & 1) * 128 + c8 * 16, s * 294912u);
        }
        VM_WAIT(0);
        if (lane == 0) st4(rw, (unsigned)s + 1u, WS_CTL + (cl_out + (kq * 9 + nj) * 4 + wave) * 128, 0);
    }
}

DEV void role_ffn1(const Params& p, LAS char* L, const int fb, const rsrc_t rw) {
    const int lane = threadIdx.x & 63, wave = __builtin_amdgcn_readfirstlane(threadIdx.x >> 6);
    const unsigned lbase = (unsigned)(size_t)L;
    const int G = lane >> 4, q4 = (lane >> 2) & 3, pp = lane & 3, n16 = lane & 15;
    u4 W[36];
    __builtin_amdgcn_s_sleep(127);
    {
        const int r0 = lane >> 2;
        const char* src = (const char*)p.w1 + (size_t)r0 * 9216 + fb * 256 + wave * 64 + (((lane & 3) ^ (((r0 >> 3) & 1) * 2)) * 16);
#pragma unroll
        for (int i = 0; i < 36; ++i) glds16(src + (size_t)i * 16 * 9216, lbase + wave * 40960 + i * 1024);
        VM_WAIT(0);
        const int rl = 8 * G + q4;
#pragma unroll
        for (int ks = 0; ks < 18; ++ks)
#pragma unroll
            for (int t = 0; t < 2; ++t) {
                LAS char* a = L + wave * 40960 + ks * 2048 + rl * 64 + (((2 * t + (pp >> 1)) ^ ((G & 1) * 2)) * 16) + (pp & 1) * 8;
                W[ks * 2 + t] = cat(trd(a), trd(a + 256));
            }
        BARRIER();
    }
    float bias[8];
#pragma unroll
    for (int i = 0; i < 8; ++i) bias[i] = bf_lo(p.b1[fb * 128 + wave * 32 + (i >> 2) * 16 + 4 * G + (i & 3)]);
    int lw[9];
#pragma unroll
    for (int i = 0; i < 9; ++i) { const int u = i * 64 + lane, row = wave * 8 + u / 72, cc = u % 72;
        lw[i] = (row >> 4) * 18432 + (cc >> 2) * 1024 + ((cc & 3) * 16 + ((row & 15) ^ (cc & 7))) * 16; }
    const int lr0 = (G * 16 + (n16 ^ G)) * 16, lr1 = (G * 16 + (n16 ^ (4 + G))) * 16;
    const unsigned vin = WS_HN + wave * 9216 + lane * 16, vdep = lane < 8 ? WS_CTL + (CL_R1 + wave * 8 + lane) * 128 : OOB;
    const unsigned vst = WS_H1 + (fb * 4 + wave) * 1024 + lane * 16;
    unsigned known = 0;
#pragma unroll 1
    for (int s = 0; s < NSEG; ++s) {
        if (known < (unsigned)s + 1u) known = wait_prog(rw, vdep, lane < 8, s + 1);
        LAS char* S = L + (s & 1) * 36864;
        u4 a[9];
#pragma unroll
        for (int i = 0; i < 9; ++i) a[i] = ld16(rw, vin + (i & 3) * 1024, s * 36864u + (i >> 2) * 4096);
#pragma unroll
        for (int i = 0; i < 9; ++i) *(LAS u4*)(S + lw[i]) = a[i];
        BARRIER();
        f4 acc[2][2];
#pragma unroll
        for (int t = 0; t < 2; ++t) { acc[0][t] = (f4){bias[4 * t], bias[4 * t + 1], bias[4 * t + 2], bias[4 * t + 3]}; acc[1][t] = acc[0][t]; }
#pragma unroll
        for (int ks = 0; ks < 18; ++ks) {
            const int lr = (ks & 1) ? lr1 : lr0;
            const u4 x0 = *(LAS u4*)(S + ks * 1024 + lr), x1 = *(LAS u4*)(S + 18432 + ks * 1024 + lr);
#pragma unroll
            for (int t = 0; t < 2; ++t) { acc[0][t] = mfma32(W[ks * 2 + t], x0, acc[0][t]); acc[1][t] = mfma32(W[ks * 2 + t], x1, acc[1][t]); }
        }
#pragma unroll
        for (int rt = 0; rt < 2; ++rt) {
            float g[8];
#pragma unroll
            for (int i = 0; i < 8; ++i) {
                const float v = acc[rt][i >> 2][i & 3];
                const float t = v * __builtin_fmaf(v * v, -0.1029432397f, -2.302208198f);
                g[i] = v * __builtin_amdgcn_rcpf(1.0f + __builtin_amdgcn_exp2f(t));
            }
            st16(rw, (u4){pk_bf16(g[0], g[1]), pk_bf16(g[2], g[3]), pk_bf16(g[4], g[5]), pk_bf16(g[6], g[7])}, vst + rt * 147456, s * 294912u);
        }
        VM_WAIT(0);
        if (lane == 0) st4(rw, (unsigned)s + 1u, WS_CTL + (CL_F1 + fb * 4 + wave) * 128, 0);
    }
}

template <bool FIRST>
DEV void role_red(const Params& p, const int j, const rsrc_t rw) {
    const int lane = threadIdx.x & 63, wave = __builtin_amdgcn_readfirstlane(threadIdx.x >> 6);
    const int r = j * 4 + wave;
    const bool third = lane < 16;
    const int cl = (FIRST ? CL_O : CL_F2) + (r >> 4) * 2;
    const unsigned vd0 = lane < 36 ? WS_CTL + (cl + lane * 4) * 128 : OOB, vd1 = lane < 36 ? vd0 + 128 : OOB;
    const unsigned vin = (FIRST ? WS_PO : WS_PF) + r * 2304 + lane * 16, vin2 = third ? vin + 2048 : OOB;
    const unsigned vrow = r * 1152 + lane * 8, vrow2 = third ? vrow + 1024 : OOB;
    float b2[12];
    if (!FIRST) {
#pragma unroll
        for (int i = 0; i < 12; ++i) b2[i] = (i < 8 || third) ? bf_lo(p.b2[(lane + 64 * (i >> 2)) * 4 + (i & 3)]) : 0.f;
    }
    unsigned k0 = 0, k1 = 0;
#pragma unroll 1
    for (int s = 0; s < NSEG; ++s) {
        u2 xr[3];
        if (FIRST) { const u2* src = (const u2*)(p.x + (s * 32 + r) * 576); xr[0] = src[lane]; xr[1] = src[lane + 64]; xr[2] = third ? src[lane + 128] : (u2){0u, 0u}; }
        if (k0 < (unsigned)s + 1u) k0 = wait_prog(rw, vd0, lane < 36, s + 1);
        if (k1 < (unsigned)s + 1u) k1 = wait_prog(rw, vd1, lane < 36, s + 1);
        if (!FIRST) { xr[0] = ld8(rw, vrow, WS_H + s * 36864u); xr[1] = ld8(rw, vrow + 512, WS_H + s * 36864u); xr[2] = ld8(rw, vrow2, WS_H + s * 36864u); }
        u4 q[4][3];
#pragma unroll
        for (int kq = 0; kq < 4; ++kq) { const unsigned so = s * 294912u + kq * 73728u; q[kq][0] = ld16(rw, vin, so); q[kq][1] = ld16(rw, vin + 1024, so); q[kq][2] = ld16(rw, vin2, so); }
        float v[12];
#pragma unroll
        for (int i = 0; i < 3; ++i) {
            const f4 t = (__builtin_bit_cast(f4, q[0][i]) + __builtin_bit_cast(f4, q[1][i])) + (__builtin_bit_cast(f4, q[2][i]) + __builtin_bit_cast(f4, q[3][i]));
            const float xv[4] = {bf_lo(xr[i].x), bf_hi(xr[i].x), bf_lo(xr[i].y), bf_hi(xr[i].y)};
#pragma unroll
            for (int e = 0; e < 4; ++e) v[4 * i + e] = FIRST ? rbf(xv[e] + rbf(t[e])) : xv[e] + (t[e] + b2[4 * i + e]);
        }
        if (FIRST) {
            st8(rw, (u2){pk_bf16(v[0], v[1]), pk_bf16(v[2], v[3])}, vrow, WS_H + s * 36864u);
            st8(rw, (u2){pk_bf16(v[4], v[5]), pk_bf16(v[6], v[7])}, vrow + 512, WS_H + s * 36864u);
            st8(rw, (u2){pk_bf16(v[8], v[9]), pk_bf16(v[10], v[11])}, vrow2, WS_H + s * 36864u);
            layernorm12(v, third);
            st8(rw, (u2){pk_bf16(v[0], v[1]), pk_bf16(v[2], v[3])}, vrow, WS_HN + s * 36864u);
            st8(rw, (u2){pk_bf16(v[4], v[5]), pk_bf16(v[6], v[7])}, vrow + 512, WS_HN + s * 36864u);
            st8(rw, (u2){pk_bf16(v[8], v[9]), pk_bf16(v[10], v[11])}, vrow2, WS_HN + s * 36864u);
            VM_WAIT(0);
            if (lane == 0) st4(rw, (unsigned)s + 1u, WS_CTL + (CL_R1 + r) * 128, 0);
        } else {
            u2* dst = (u2*)(p.out + (s * 32 + r) * 576);
            dst[lane] = (u2){pk_bf16(v[0], v[1]), pk_bf16(v[2], v[3])};
            dst[lane + 64] = (u2){pk_bf16(v[4], v[5]), pk_bf16(v[6], v[7])};
            if (third) dst[lane + 128] = (u2){pk_bf16(v[8], v[9]), pk_bf16(v[10], v[11])};
        }
    }
}

DEV void role_probe(const rsrc_t rw) {
    if (threadIdx.x >= 64) return;
    unsigned sel = 8u;
    const unsigned far = (xcc_id() >> 2) & 1u;
#pragma unroll 1
    for (int b = 0; b < 3; ++b) {
        unsigned best0 = 0xffffffffu, best1 = 0xffffffffu;
        const unsigned base = b == 2 ? WS_MEM + 16384u + 4096u : (b == 0 ? WS_PART0 : WS_PART1) + 0x180000u;
#pragma unroll 1
        for (int i = 0; i < 6; ++i) {
            const unsigned long long t0 = __builtin_amdgcn_s_memtime();
            if (threadIdx.x == 0) st4(rw, 0u, base + (i & 1) * 8192u, 0);
            VM_WAIT(0);
            const unsigned d = (unsigned)(__builtin_amdgcn_s_memtime() - t0);
            if (i & 1) best1 = min(best1, d); else best0 = min(best0, d);
        }
        sel |= ((best1 < best0 ? 1u : 0u) ^ far) << b;
    }
    if (threadIdx.x == 0) st4(rw, sel, WS_CTL + CL_SEL * 128, 0);
}

__global__ void __launch_bounds__(256, 1) k_main(Params p) {
    __shared__ __attribute__((aligned(16))) char lds[163840];
    LAS char* L = (LAS char*)lds;
    const rsrc_t rw = __builtin_amdgcn_make_buffer_rsrc(p.ws, 0, WS_END, 0x00020000);
    int role;
    if (p.rolemask != 0xffu) role = (blockIdx.x & 7) * 32 + (blockIdx.x >> 3);
    else {
        if (threadIdx.x == 0) {
            unsigned r = xcc_id() * 32 + (blockIdx.x >> 3);
            for (int i = 0; i < 256; ++i, r = (r + 1) & 255u)
                if (__hip_atomic_exchange((GAS unsigned*)(p.ws + WS_CTL + (CL_CLAIM + r) * 128), 1u, RLX_AGENT) == 0u) break;
            *(LAS unsigned*)L = r;
        }
        BARRIER();
        role = __builtin_amdgcn_readfirstlane(*(LAS unsigned*)L);
        BARRIER();
    }
    const int xc = role >> 5, ix = role & 31, lane = threadIdx.x & 63, wave = __builtin_amdgcn_readfirstlane(threadIdx.x >> 6);
    if (xc < 4) {
        if (ix < 16) { if (p.rolemask & 2u) role_chain(p, L, xc * 16 + ix, rw); }
        else if (p.rolemask & 1u) role_qkv(p, L, xc * 16 + ix - 16, rw);
    } else {
        const int kq = xc - 4;
        if (ix < 9) { if (p.rolemask & 4u) role_proj<8>(L, p.wo, kq, ix, rw, WS_O, WS_PO, lane < 8 ? WS_CTL + (CL_ATT + (kq * 16 + wave * 4) * 2 + lane) * 128 : OOB, lane < 8, CL_O); }
        else if (ix < 18) { if (p.rolemask & 16u) role_ffn1(p, L, kq * 9 + ix - 9, rw); }
        else if (ix < 27) { if (p.rolemask & 32u) role_proj<9>(L, p.w2, kq, ix - 18, rw, WS_H1, WS_PF, lane < 9 ? WS_CTL + (CL_F1 + (kq * 4 + wave) * 9 + lane) * 128 : OOB, lane < 9, CL_F2); }
        else if (ix < 29) { if (p.rolemask & 8u) role_red<true>(p, kq * 2 + ix - 27, rw); }
        else if (ix < 31) { if (p.rolemask & 64u) role_red<false>(p, kq * 2 + ix - 29, rw); }
        else if (kq == 0 && (p.rolemask & 2u)) role_probe(rw);
    }
}

extern "C" void kernel_launch(void* const* d_in, const int* in_sizes, int n_in, void* d_out, int out_size, void* d_ws, size_t ws_size,
                              hipStream_t stream) {
    Params p;
    p.x = (const unsigned short*)d_in[0]; p.mem0 = (const unsigned short*)d_in[1]; p.wq = (const unsigned short*)d_in[2];
    p.wk = (const unsigned short*)d_in[3]; p.wv = (const unsigned short*)d_in[4]; p.wo = (const unsigned short*)d_in[5];
    p.w1 = (const unsigned short*)d_in[8]; p.b1 = (const unsigned short*)d_in[9]; p.w2 = (const unsigned short*)d_in[10];
    p.b2 = (const unsigned short*)d_in[11]; p.out = (unsigned short*)d_out;
    p.ws = (char*)(((size_t)d_ws + (2u << 20) - 1) & ~(size_t)((2u << 20) - 1));
    p.rolemask = 0xffu; p.pad0 = 0;
    k_init<<<256, 256, 0, stream>>>(p);
    k_main<<<256, 256, 0, stream>>>(p);
}
```

```cpp
#include <hip/hip_runtime.h>
#include <hip/hip_bf16.h>

typedef unsigned u2 __attribute__((ext_vector_type(2)));
typedef unsigned u4 __attribute__((ext_vector_type(4)));
typedef unsigned u8v __attribute__((ext_vector_type(8)));
typedef float f4 __attribute__((ext_vector_type(4)));
typedef __bf16 bf2 __attribute__((ext_vector_type(2)));
typedef __bf16 bf4 __attribute__((ext_vector_type(4)));
typedef __bf16 bf8 __attribute__((ext_vector_type(8)));
typedef float f2 __attribute__((ext_vector_type(2)));
#define LAS __attribute__((address_space(3)))
#define GAS __attribute__((address_space(1)))
#define DEV __device__ __forceinline__
#define RLX_AGENT __ATOMIC_RELAXED, __HIP_MEMORY_SCOPE_AGENT

#define DELAY1 21
#define RPRE "s_nop 15\n\ts_nop 15\n\t"
#define DTOL 5
#define DSTAG 3
#define RSLEEP 2
#define CLEAD 42
#define DEXTRA 35
#define PSLEEP "1"
#define RESETAT 36
#define DBUMP 2
#define WSLEEP 2
#define VETO 0x15u
#define VLEAD 750
#define DELAY2 58

constexpr int NSEG = 64;
constexpr unsigned SENT = 0xFFFFFFFFu;
constexpr unsigned MB = 1u << 20;
constexpr unsigned WS_PART = 32 * MB, WS_MEM = 38 * MB;
constexpr unsigned LA_QKV = 0, LA_O = 786432;
constexpr unsigned LB_H1 = 0, LB_PO = 294912, LB_PF = 2 * 294912, LB_H = 3 * 294912, LB_HN = LB_H + 36864;
constexpr unsigned WS_CTL = 64 * MB;
constexpr unsigned WS_XN = 65 * MB;
constexpr unsigned WS_END = 68 * MB;
constexpr int CL_ABORT = 0, CL_SEL = 1, CL_GO = 2, CL_QKV = 8, CL_ATT = CL_QKV + 64, CL_R1 = CL_ATT + 128, CL_F1 = CL_R1 + 32,
              CL_RDY = CL_F1 + 192, CL_XN = CL_RDY + 64, CL_RED = CL_XN + 64, CL_ARM = CL_RED + 96, CL_END = CL_ARM + 72,
              CL_EPOCH = 768, CL_INIT = 769, CL_GO8 = 770, CL_CLAIM = 772;
constexpr int RING = 16, RINGS = 8;
constexpr int SPLIT = 54;
__host__ __device__ constexpr unsigned pm(unsigned off) { return ((off >> 13) << 14) | (off & 8191u); }
constexpr unsigned OOB = 0x7fffffffu;
constexpr unsigned SPIN_CAP = 1u << 18;

struct Params {
    const unsigned short* x; const unsigned short* mem0; const unsigned short* wq; const unsigned short* wk;
    const unsigned short* wv; const unsigned short* wo; const unsigned short* w1; const unsigned short* b1;
    const unsigned short* w2; const unsigned short* b2; unsigned short* out; char* ws;
    unsigned rolemask; unsigned pad0;
};

#define ST_DECL
#define ST(k)
#define STC(k)
#define STF(k)
#define STN(k)
#define STR(k)
#define STQ(k)
#define ST_FLUSH(ws, role, s)
#define CNT(k, v)
#define MARK(ws, i)
typedef __amdgpu_buffer_rsrc_t rsrc_t;
DEV u4 ld16(rsrc_t r, unsigned vo, unsigned so) { return __builtin_amdgcn_raw_buffer_load_b128(r, vo, so, 16); }
DEV u2 ld8(rsrc_t r, unsigned vo, unsigned so) { return __builtin_amdgcn_raw_buffer_load_b64(r, vo, so, 16); }
DEV unsigned ld4(rsrc_t r, unsigned vo, unsigned so) { return __builtin_amdgcn_raw_buffer_load_b32(r, vo, so, 16); }
DEV void st16(rsrc_t r, u4 v, unsigned vo, unsigned so) {
    __builtin_amdgcn_raw_buffer_store_b128(v, r, vo, so, 16);
    asm volatile("s_nop 1" :: "v"(v) : "memory");
}
DEV void st8(rsrc_t r, u2 v, unsigned vo, unsigned so) { __builtin_amdgcn_raw_buffer_store_b64(v, r, vo, so, 16); }
DEV void st4(rsrc_t r, unsigned v, unsigned vo, unsigned so) { __builtin_amdgcn_raw_buffer_store_b32(v, r, vo, so, 16); }
#define RFL(x) ((unsigned)__builtin_amdgcn_readfirstlane(x))
#define CBAR() asm volatile("" ::: "memory")
#define VM_WAIT(n) asm volatile("s_waitcnt vmcnt(" #n ")" ::: "memory")
#define BARRIER() do { asm volatile("s_waitcnt lgkmcnt(0)" ::: "memory"); __builtin_amdgcn_s_barrier(); CBAR(); } while (0)
#define SCHED() __builtin_amdgcn_sched_barrier(0)

DEV unsigned pk_bf16(float lo, float hi) { const bf2 v = __builtin_convertvector((f2){lo, hi}, bf2); return __builtin_bit_cast(unsigned, v); }
DEV float bf_lo(unsigned u) { return __uint_as_float(u << 16); }
DEV float bf_hi(unsigned u) { return __uint_as_float(u & 0xffff0000u); }
DEV float rbf(float v) { return bf_lo(pk_bf16(v, v)); }
DEV unsigned xcc_id() { return (unsigned)__builtin_amdgcn_s_getreg(20 | (3 << 11)) & 7u; }
DEV void glds16(const void* gsrc, unsigned lds_dst) {
    unsigned keep;
    asm volatile("s_mov_b32 %0, m0\n\ts_mov_b32 m0, %2\n\ts_nop 0\n\tglobal_load_lds_dwordx4 %1, off\n\ts_mov_b32 m0, %0"
                 : "=&s"(keep) : "v"(gsrc), "s"(lds_dst) : "memory");
}
DEV u2 trd(LAS char* p) { return __builtin_bit_cast(u2, __builtin_amdgcn_ds_read_tr16_b64_v4bf16((LAS bf4*)p)); }
DEV u4 cat(u2 a, u2 b) { return (u4){a.x, a.y, b.x, b.y}; }
DEV u8v cat(u4 a, u4 b) { return __builtin_shufflevector(a, b, 0, 1, 2, 3, 4, 5, 6, 7); }
DEV f4 mfma32(u4 a, u4 b, f4 c) { return __builtin_amdgcn_mfma_f32_16x16x32_bf16(__builtin_bit_cast(bf8, a), __builtin_bit_cast(bf8, b), c, 0, 0, 0); }
DEV float dpp_ror(float v, int n) {
    const int i = __float_as_int(v);
    return __int_as_float(n == 8 ? __builtin_amdgcn_update_dpp(i, i, 0x128, 0xf, 0xf, false) : n == 4 ? __builtin_amdgcn_update_dpp(i, i, 0x124, 0xf, 0xf, false)
                        : n == 2 ? __builtin_amdgcn_update_dpp(i, i, 0x122, 0xf, 0xf, false) : __builtin_amdgcn_update_dpp(i, i, 0x121, 0xf, 0xf, false));
}
DEV float xg_sum(float v);
DEV float wave_sum(float v) { v += dpp_ror(v, 8); v += dpp_ror(v, 4); v += dpp_ror(v, 2); v += dpp_ror(v, 1); return xg_sum(v); }
DEV float swap_add(float a, float b) {
    const auto r = __builtin_amdgcn_permlane32_swap(__float_as_uint(a), __float_as_uint(b), false, false);
    return __uint_as_float(r[0]) + __uint_as_float(r[1]);
}
DEV float xg_max(float v) {
    auto r = __builtin_amdgcn_permlane16_swap(__float_as_uint(v), __float_as_uint(v), false, false);
    v = fmaxf(__uint_as_float(r[0]), __uint_as_float(r[1]));
    r = __builtin_amdgcn_permlane32_swap(__float_as_uint(v), __float_as_uint(v), false, false);
    return fmaxf(__uint_as_float(r[0]), __uint_as_float(r[1]));
}
DEV float xg_sum(float v) {
    auto r = __builtin_amdgcn_permlane16_swap(__float_as_uint(v), __float_as_uint(v), false, false);
    v = __uint_as_float(r[0]) + __uint_as_float(r[1]);
    r = __builtin_amdgcn_permlane32_swap(__float_as_uint(v), __float_as_uint(v), false, false);
    return __uint_as_float(r[0]) + __uint_as_float(r[1]);
}
DEV bool spin_out(unsigned& spins, rsrc_t rw) {
    ++spins;
    if ((spins & 1023u) != 0u) return false;
    if (spins >= SPIN_CAP) { st4(rw, 1u, WS_CTL + CL_ABORT * 128, 0); return true; }
    return __builtin_amdgcn_readfirstlane(ld4(rw, WS_CTL + CL_ABORT * 128, 0)) != 0u;
}
DEV unsigned row_min_u32(unsigned v) {
    v = min(v, (unsigned)__builtin_amdgcn_update_dpp(v, v, 0x128, 0xf, 0xf, false)); v = min(v, (unsigned)__builtin_amdgcn_update_dpp(v, v, 0x124, 0xf, 0xf, false));
    v = min(v, (unsigned)__builtin_amdgcn_update_dpp(v, v, 0x122, 0xf, 0xf, false)); v = min(v, (unsigned)__builtin_amdgcn_update_dpp(v, v, 0x121, 0xf, 0xf, false));
    return v;
}
DEV unsigned min16(unsigned v) { return RFL(row_min_u32(v)); }
DEV unsigned min64(unsigned v) {
    v = row_min_u32(v);
    return min(min((unsigned)__builtin_amdgcn_readlane(v, 0), (unsigned)__builtin_amdgcn_readlane(v, 16)), min((unsigned)__builtin_amdgcn_readlane(v, 32), (unsigned)__builtin_amdgcn_readlane(v, 48)));
}
template <int SLP = 1> DEV unsigned poll_prog(rsrc_t rw, unsigned vo, unsigned need) {
    unsigned v, n = 0;
    asm volatile("1:\n\tbuffer_load_dword %0, %2, %3, 0 offen sc1\n\ts_add_u32 %1, %1, 1\n\ts_cmp_lt_u32 %1, 0x40000\n\ts_waitcnt vmcnt(0)\n\tv_cmp_gt_u32 vcc, %4, %0\n\t"
                 "s_cbranch_vccz 2f\n\ts_sleep %c5\n\ts_cbranch_scc1 1b\n\tv_mov_b32 %0, 64\n2:"
                 : "=&v"(v), "+s"(n) : "v"(vo), "s"(rw), "s"(need), "i"(SLP) : "vcc", "scc", "memory");
    return v;
}
DEV void wait_init(rsrc_t rw, u2 tag) {
    for (unsigned n = 0; n < SPIN_CAP; ++n) { CBAR(); const u2 v = ld8(rw, WS_CTL + CL_INIT * 128, 0); if (__all(v.x == tag.x && v.y == tag.y)) break; __builtin_amdgcn_s_sleep(4); }
}
DEV unsigned get_sel(rsrc_t rw, u2 tag) {
    wait_init(rw, tag);
    const unsigned raw = (unsigned)__ballot(poll_prog<2>(rw, WS_CTL + CL_SEL * 128 + (threadIdx.x & 31) * 4, 2) & 1u);
    const int d = __builtin_popcount(raw ^ 0x55555555u);
    return d <= 9 ? 0x55555555u : d >= 23 ? 0xaaaaaaaau : raw;
}
DEV unsigned wait_go(rsrc_t rw, u2 tag) {
    u2 v = {0u, 0u};
    for (unsigned n = 0; n < SPIN_CAP; ++n) { CBAR(); v = ld8(rw, WS_CTL + CL_GO8 * 128, 0); if (__all(v.x == (tag.x ^ tag.y))) break; __builtin_amdgcn_s_sleep(4); }
    return RFL(v.y);
}
DEV void touch_blocks(rsrc_t rw) { const unsigned t_ = ld4(rw, min((int)(threadIdx.x & 63), 33) * 2 * MB + 0x1ff000u, 0); asm volatile("" :: "v"(t_)); }
DEV unsigned far_a(unsigned sel, int s) { const unsigned i = s & (RING - 1); return i * 2 * MB + ((~sel >> i) & 1u) * 8192u; }
DEV unsigned far_b(unsigned sel, int s) { const unsigned i = 16 + (s & (RING - 1)); return i * 2 * MB + ((~sel >> i) & 1u) * 8192u; }
DEV unsigned far_s(unsigned sel, int s) { return far_b(sel, s & (RINGS - 1)); }
DEV void probe_block(rsrc_t rw, int b, u2 tag) {
    unsigned best0 = 0xffffffffu, best1 = 0xffffffffu;
    const unsigned base = b * 2 * MB + (b == 19 ? 16384u + 6144u : 0x180000u);
#pragma unroll 1
    for (int i = 0; i < 8; ++i) {
        const unsigned long long t0 = __builtin_amdgcn_s_memtime();
        if ((threadIdx.x & 63) == 0) st4(rw, 0u, base + (i & 1) * 8192u + (i >> 1) * 256u, 0);
        VM_WAIT(0);
        const unsigned d = (unsigned)(__builtin_amdgcn_s_memtime() - t0);
        if (i & 1) best1 = min(best1, d); else best0 = min(best0, d);
    }
    wait_init(rw, tag);
    if ((threadIdx.x & 63) == 0) st4(rw, 2u | ((best1 < best0 ? 1u : 0u) ^ ((xcc_id() >> 2) & 1u)), WS_CTL + CL_SEL * 128 + b * 4, 0);
}
DEV void layernorm12(float (&v)[12], bool third) {
    float s = 0.f;
#pragma unroll
    for (int i = 0; i < 12; ++i) s += v[i];
    const float mean = wave_sum(s) * (1.0f / 576.0f);
    float q = 0.f;
#pragma unroll
    for (int i = 0; i < 12; ++i) { const float d = (i < 8 || third) ? v[i] - mean : 0.f; q += d * d; }
    const float var = wave_sum(q) * (1.0f / 576.0f);
    const float mb = rbf(mean), rs = __builtin_amdgcn_rcpf(rbf(__builtin_amdgcn_sqrtf(rbf(rbf(var) + 1e-5f))));
#pragma unroll
    for (int i = 0; i < 12; ++i) v[i] = rbf(v[i] - mb) * rs;
}

DEV void ln_segment(const Params& p, LAS char* L, rsrc_t rw, int seg, u2 tag) {
    const int lane = threadIdx.x & 63, wave = threadIdx.x >> 6;
    const int row = wave * 8 + (lane >> 3), j = lane & 7;
    const u4* src = (const u4*)(p.x + (seg * 32 + row) * 576);
    u4 t[9];
#pragma unroll
    for (int i = 0; i < 9; ++i) t[i] = src[i * 8 + j];
    MARK(p.ws, 192 + seg);
    auto sum8 = [&](float v) {
        v += __int_as_float(__builtin_amdgcn_update_dpp(0, __float_as_int(v), 0xB1, 0xf, 0xf, false));
        v += __int_as_float(__builtin_amdgcn_update_dpp(0, __float_as_int(v), 0x4E, 0xf, 0xf, false));
        return v + __int_as_float(__builtin_amdgcn_update_dpp(0, __float_as_int(v), 0x141, 0xf, 0xf, false));
    };
    float s1 = 0.f;
#pragma unroll
    for (int i = 0; i < 9; ++i)
#pragma unroll
        for (int e = 0; e < 4; ++e) s1 += bf_lo(t[i][e]) + bf_hi(t[i][e]);
    const float mean = sum8(s1) * (1.0f / 576.0f);
    float s2 = 0.f;
#pragma unroll
    for (int i = 0; i < 9; ++i)
#pragma unroll
        for (int e = 0; e < 4; ++e) { const float a = bf_lo(t[i][e]) - mean, b = bf_hi(t[i][e]) - mean; s2 += a * a + b * b; }
    const float var = sum8(s2) * (1.0f / 576.0f);
    const float mb = rbf(mean), rs = __builtin_amdgcn_rcpf(rbf(__builtin_amdgcn_sqrtf(rbf(rbf(var) + 1e-5f))));
#pragma unroll
    for (int i = 0; i < 9; ++i) {
        u4 o;
#pragma unroll
        for (int e = 0; e < 4; ++e) { const unsigned d = pk_bf16(bf_lo(t[i][e]) - mb, bf_hi(t[i][e]) - mb); o[e] = pk_bf16(bf_lo(d) * rs, bf_hi(d) * rs); }
        const int cc = i * 8 + j;
        LAS char* dst = L + (row >> 4) * 18432 + (cc >> 2) * 1024 + (((2 * cc) & 3) * 16 + (row & 15)) * 16 + ((cc >> 1) & 1) * 8;
        *(LAS u2*)dst = (u2){o.x, o.y}; *(LAS u2*)(dst + 256) = (u2){o.z, o.w};
    }
    MARK(p.ws, 128 + seg);
    BARRIER();
#pragma unroll
    for (int i = 0; i < 9; ++i) st16(rw, *(LAS u4*)(L + i * 4096 + threadIdx.x * 16), i * 4096 + threadIdx.x * 16, WS_XN + seg * 36864);
    VM_WAIT(0);
    BARRIER();
    wait_init(rw, tag);
    if (threadIdx.x == 0) st4(rw, 1u, WS_CTL + (CL_XN + seg) * 128, 0);
    MARK(p.ws, seg);
}

#define AL8
#define SMF(acc, a, b, idx, bc) asm volatile(AL8 "v_smfmac_f32_16x16x64_bf16 %0, %1, %2, %3" : "+v"(acc) : "v"(a), bc(b), "v"(idx))
#define NOP2() asm volatile("s_nop 1")

template <int I> DEV void smf_w(f4& acc, const u4& a, const u8v& w, unsigned idx) {
    if constexpr (I < 32) SMF(acc, a, w, idx, "a"); else SMF(acc, a, w, idx, "v");
}
DEV void mf32(f4& d, const u4& a, const u4& b) { asm volatile(AL8 "v_mfma_f32_16x16x32_bf16 %0, %1, %2, %0" : "+v"(d) : "v"(a), "v"(b)); }
DEV void mf32z(f4& d, const u4& a, const u4& b) { asm volatile(AL8 "v_mfma_f32_16x16x32_bf16 %0, %1, %2, 0" : "=&v"(d) : "v"(a), "v"(b)); }
DEV void mf32c(f4& d, const u4& a, const u4& b, const f4& c) { asm volatile(AL8 "v_mfma_f32_16x16x32_bf16 %0, %1, %2, %3" : "=&v"(d) : "v"(a), "v"(b), "v"(c)); }
DEV void mf16(f4& d, const u2& a, const u2& b) { asm volatile(AL8 "v_mfma_f32_16x16x16_bf16 %0, %1, %2, %0" : "+v"(d) : "v"(a), "v"(b)); }

template <int N, class Fn> DEV void sfor(Fn&& f) { if constexpr (N > 0) { sfor<N - 1>(f); f(std::integral_constant<int, N - 1>{}); } }

DEV float max3(float a, float b, float c) { float r; asm("v_max3_f32 %0, %1, %2, %3" : "=v"(r) : "v"(a), "v"(b), "v"(c)); return r; }
DEV float max8(const f4& a, const f4& b) {
    float r, t;
    asm("v_max3_f32 %0, %2, %3, %4\n\tv_max3_f32 %1, %5, %6, %7\n\tv_max3_f32 %0, %0, %8, %9\n\tv_max_f32 %0, %0, %1"
        : "=&v"(r), "=&v"(t) : "v"(a[0]), "v"(a[1]), "v"(a[2]), "v"(a[3]), "v"(b[0]), "v"(b[1]), "v"(b[2]), "v"(b[3]));
    return r;
}
DEV float xmax16(float v) { float t; asm("v_mov_b32 %1, %0\n\ts_nop 1\n\tv_permlane16_swap_b32 %0, %1\n\tv_max_f32 %0, %0, %1" : "+v"(v), "=&v"(t)); return v; }
DEV float xmax32(float v) { float t; asm("v_mov_b32 %1, %0\n\ts_nop 1\n\tv_permlane32_swap_b32 %0, %1\n\tv_max_f32 %0, %0, %1" : "+v"(v), "=&v"(t)); return v; }
DEV float max5x16(float m, const f4& v) {
    float t;
    asm("v_max3_f32 %0, %0, %2, %3\n\tv_max3_f32 %0, %0, %4, %5\n\tv_mov_b32 %1, %0\n\ts_nop 1\n\tv_permlane16_swap_b32 %0, %1\n\tv_max_f32 %0, %0, %1"
        : "+v"(m), "=&v"(t) : "v"(v[0]), "v"(v[1]), "v"(v[2]), "v"(v[3]));
    return m;
}
DEV u2 exp4(const f4& s, float c, float mc) {
    const f4 t = s * c - mc;
    return (u2){pk_bf16(__builtin_amdgcn_exp2f(t[0]), __builtin_amdgcn_exp2f(t[1])), pk_bf16(__builtin_amdgcn_exp2f(t[2]), __builtin_amdgcn_exp2f(t[3]))};
}
DEV u4 norm_pack(const f4& a, const f4& b, float inv) {
    const f4 x = a * inv, y = b * inv;
    return (u4){pk_bf16(x[0], x[1]), pk_bf16(x[2], x[3]), pk_bf16(y[0], y[1]), pk_bf16(y[2], y[3])};
}
DEV void spin_cycles(int rem) {
    asm volatile("1:\n\ts_sub_i32 %0, %0, 32\n\ts_nop 0\n\ts_cmp_gt_i32 %0, 0\n\ts_cbranch_scc1 1b" : "+s"(rem) :: "scc");
}
DEV f4 mkz() { f2 lo, hi; asm volatile("v_mov_b64 %0, 0\n\tv_mov_b64 %1, 0" : "=v"(lo), "=v"(hi)); return __builtin_shufflevector(lo, hi, 0, 1, 2, 3); }
struct Clk { unsigned rt, ct, cpt, tpc; };
DEV void clk_edge(Clk& k) {
    unsigned r0, r1, c1, n = 0;
    asm volatile("s_memrealtime s[96:97]\n\ts_waitcnt lgkmcnt(0)\n\ts_mov_b32 %0, s96\n"
                 "1:\n\ts_memrealtime s[96:97]\n\ts_memtime s[98:99]\n\ts_add_u32 %3, %3, 1\n\ts_cmp_gt_u32 %3, 64\n\ts_cbranch_scc1 2f\n\ts_waitcnt lgkmcnt(0)\n\ts_cmp_eq_u32 %0, s96\n\ts_cbranch_scc1 1b\n"
                 "2:\n\ts_waitcnt lgkmcnt(0)\n\ts_mov_b32 %1, s96\n\ts_mov_b32 %2, s98"
                 : "=&s"(r0), "=&s"(r1), "=&s"(c1), "+s"(n) :: "s96", "s97", "s98", "s99", "scc", "memory");
    const unsigned dr = r1 - k.rt, dc = c1 - k.ct;
    if (dr >= 64u && dr < 4096u) {
        const float fc = (float)dc, fr = (float)dr;
        unsigned x = (unsigned)(fc * __builtin_amdgcn_rcpf(fr) * 256.0f), y = (unsigned)(fr * __builtin_amdgcn_rcpf(fc) * 4294967296.0f);
        asm volatile("s_nop 0\n\tv_readfirstlane_b32 %0, %2\n\tv_readfirstlane_b32 %1, %3\n\ts_nop 4" : "=&s"(k.cpt), "=&s"(k.tpc) : "v"(x), "v"(y));
    }
    k.rt = r1; k.ct = c1;
}
DEV unsigned clk_tick(const Clk& k, unsigned cyc) { return k.rt + __umulhi(cyc - k.ct, k.tpc); }
DEV void clk_wait(const Clk& k, unsigned tick, int lead) {
    const unsigned tc = __builtin_amdgcn_readfirstlane(k.ct + (unsigned)((((int)(tick - k.rt) * (int)k.cpt) >> 8) - lead));
    unsigned r, n = 0;
    asm volatile("1:\n\ts_memtime s[98:99]\n\ts_add_u32 %1, %1, 1\n\ts_cmp_gt_u32 %1, 400\n\ts_cbranch_scc1 3f\n\ts_waitcnt lgkmcnt(0)\n\ts_sub_u32 %0, %2, s98\n\ts_cmp_gt_i32 %0, 150\n\ts_cbranch_scc1 1b\n\t"
                 "s_sub_u32 %0, %0, 60\n\ts_cmp_gt_i32 %0, 0\n\ts_cbranch_scc0 3f\n"
                 "2:\n\ts_sub_u32 %0, %0, 24\n\ts_cbranch_scc0 2b\n"
                 "3:\n\ts_waitcnt lgkmcnt(0)" : "=&s"(r), "+s"(n) : "s"(tc) : "s98", "s99", "scc");
}
DEV int wave_max_i32(int v) {
    v = max(v, __builtin_amdgcn_update_dpp(v, v, 0x128, 0xf, 0xf, false)); v = max(v, __builtin_amdgcn_update_dpp(v, v, 0x124, 0xf, 0xf, false));
    v = max(v, __builtin_amdgcn_update_dpp(v, v, 0x122, 0xf, 0xf, false)); v = max(v, __builtin_amdgcn_update_dpp(v, v, 0x121, 0xf, 0xf, false));
    return max(max(__builtin_amdgcn_readlane(v, 0), __builtin_amdgcn_readlane(v, 16)), max(__builtin_amdgcn_readlane(v, 32), __builtin_amdgcn_readlane(v, 48)));
}
DEV unsigned latest_stamp(rsrc_t rw, unsigned so, unsigned base, unsigned& spins) {
    const unsigned vo = (threadIdx.x & 63) * 64 + 1024;
    unsigned v;
    for (;;) { CBAR(); v = ld4(rw, vo, so); if (!__any(v == SENT) || spin_out(spins, rw)) break; }
    return base + (unsigned)wave_max_i32((int)(v - base));
}
struct Sched { unsigned ts, w; };
DEV void sched_next(const Params& p, Sched& h, unsigned tr, int s) {
    const unsigned tp = h.ts + (h.w >> 2);
    const bool late = s < 2 || (int)(tr - tp) > DTOL;
    if (s < 2) h.w = (tr - h.ts) * 4u;
    else h.w = late ? h.w + 4u : (int)(tr - tp) < -8 ? (tr - h.ts) * 4u : h.w + (tr - tp);
    h.ts = late ? tr + DELAY2 + DEXTRA : tp + DELAY2;
}
DEV unsigned now32() { return (unsigned)__builtin_amdgcn_s_memtime(); }

constexpr int C_WO = 0, C_QK = 73728, C_VM = C_QK + 4352, C_KX = C_VM + 2048, C_WV = C_KX + 40960, C_RC = C_WV + 40960, C_MK = C_RC + 512, C_TW = C_MK + 1056;

#define CHAIN_STEP \
    "1:\nv_add_u32 v240, s36, v8\n\tv_add_u32 v241, s36, v19\n\tv_mov_b32 v242, 0\n\tv_mov_b64 v[124:125], 0\n\tv_mov_b64 v[126:127], 0\n\tv_mov_b64 v[128:129], 0\n\tv_mov_b64 v[130:131], 0\n\t" \
    "v_mov_b64 v[132:133], 0\n\tv_mov_b64 v[134:135], 0\n\tv_mov_b64 v[136:137], 0\n\tv_mov_b64 v[138:139], 0\n\tv_mov_b64 v[140:141], 0\n\tv_mov_b64 v[142:143], 0\n\ts_add_u32 s96, s37, 0x1000\n\t" \
    "s_add_u32 s97, s37, 0x4000\n\ts_cmp_eq_u32 s75, 0\n\ts_cselect_b32 s89, s97, 0x40000000\n\ts_mov_b32 s87, 0\n\ts_cmp_eq_u32 s38, 0\n\ts_cbranch_scc1 12f\n\ts_mov_b32 s95, 0\n\t10:\n" \
    "s_memtime s[98:99]\n\ts_memrealtime s[8:9]\n\ts_add_u32 s95, s95, 1\n\ts_cmp_gt_u32 s95, 400\n\ts_cbranch_scc1 12f\n\ts_waitcnt lgkmcnt(0)\n\ts_sub_u32 s94, s38, s98\n\ts_sub_u32 s8, s72, s8\n\t" \
    "s_mul_i32 s8, s8, s51\n\ts_ashr_i32 s8, s8, 8\n\ts_min_i32 s94, s94, s8\n\ts_sub_u32 s8, s8, 96\n\ts_max_i32 s94, s94, s8\n\ts_cmp_gt_i32 s94, 258\n\ts_cbranch_scc1 10b\n\t" \
    "s_sub_u32 s94, s94, 148\n\ts_cmp_gt_i32 s94, 0\n\ts_cbranch_scc0 12f\n\t11:\ns_sub_u32 s94, s94, 24\n\ts_cbranch_scc0 11b\n\t12:\ns_waitcnt lgkmcnt(0)\n\ts_nop 15\n\ts_barrier\n\t15:\n" \
    "buffer_load_dwordx4 v[88:91], v10, %[rs], s37 offen offset:0 sc1\n\tbuffer_load_dwordx4 v[92:95], v10, %[rs], s37 offen offset:1024 sc1\n\tds_read_b128 v[192:195], v20 offset:0\n\t" \
    "ds_read_b128 v[196:199], v20 offset:1024\n\tds_read_b128 v[200:203], v20 offset:2048\n\tbuffer_load_dwordx4 v[96:99], v10, %[rs], s37 offen offset:2048 sc1\n\t" \
    "ds_read_b128 v[204:207], v20 offset:3072\n\tds_read_b128 v[208:211], v20 offset:4096\n\tds_read_b128 v[212:215], v20 offset:5120\n\t" \
    "buffer_load_dwordx4 v[100:103], v10, %[rs], s37 offen offset:3072 sc1\n\tds_read_b128 v[216:219], v20 offset:6144\n\tds_read_b128 v[220:223], v20 offset:7168\n\t" \
    "ds_read_b128 v[224:227], v20 offset:8192\n\tbuffer_load_dwordx4 v[104:107], v10, %[rs], s96 offen offset:0 sc1\n\tds_read_b128 v[228:231], v20 offset:9216\n\t" \
    "ds_read_b128 v[144:147], v240 offset:0\n\tds_read_b128 v[160:163], v240 offset:4096\n\tbuffer_load_dwordx4 v[108:111], v10, %[rs], s96 offen offset:1024 sc1\n\t" \
    "ds_read_b128 v[148:151], v240 offset:1024\n\tds_read_b128 v[164:167], v240 offset:5120\n\tds_read_b128 v[152:155], v240 offset:2048\n\t" \
    "buffer_load_dwordx4 v[112:115], v10, %[rs], s96 offen offset:2048 sc1\n\tds_read_b128 v[168:171], v240 offset:6144\n\tds_read_b128 v[156:159], v240 offset:3072\n\t" \
    "ds_read_b128 v[172:175], v240 offset:7168\n\tbuffer_load_dwordx4 v[116:119], v10, %[rs], s96 offen offset:3072 sc1\n\tds_read_b64_tr_b16 v[176:177], v241 offset:0\n\t" \
    "ds_read_b64_tr_b16 v[178:179], v241 offset:2048\n\tds_read_b64_tr_b16 v[180:181], v241 offset:8\n\tbuffer_load_dwordx4 v[120:123], v10, %[rs], s97 offen offset:0 sc1\n\t" \
    "ds_read_b64_tr_b16 v[182:183], v241 offset:2056\n\tds_read_b64_tr_b16 v[184:185], v241 offset:1024\n\tds_read_b64_tr_b16 v[186:187], v241 offset:3072\n\t" \
    "ds_read_b64_tr_b16 v[188:189], v241 offset:1032\n\tds_read_b64_tr_b16 v[190:191], v241 offset:3080\n\ts_waitcnt vmcnt(8)\n\tv_smfmac_f32_16x16x64_bf16 v[124:127], v[88:91], a[0:7], v12\n\t" \
    "v_max3_u32 v242, v88, v89, v242\n\tv_smfmac_f32_16x16x64_bf16 v[128:131], v[88:91], a[8:15], v12\n\tv_max3_u32 v242, v90, v91, v242\n\t" \
    "v_smfmac_f32_16x16x64_bf16 v[132:135], v[88:91], a[16:23], v12\n\tv_smfmac_f32_16x16x64_bf16 v[136:139], v[88:91], a[24:31], v12\n\t" \
    "v_smfmac_f32_16x16x64_bf16 v[140:143], v[88:91], v[56:63], v12\n\ts_waitcnt vmcnt(7)\n\tv_mov_b64 v[248:249], 0\n\tv_smfmac_f32_16x16x64_bf16 v[124:127], v[92:95], a[32:39], v12\n\t" \
    "v_max3_u32 v242, v92, v93, v242\n\tv_smfmac_f32_16x16x64_bf16 v[128:131], v[92:95], a[40:47], v12\n\tv_max3_u32 v242, v94, v95, v242\n\t" \
    "v_smfmac_f32_16x16x64_bf16 v[132:135], v[92:95], a[48:55], v12\n\tv_smfmac_f32_16x16x64_bf16 v[136:139], v[92:95], a[56:63], v12\n\t" \
    "v_smfmac_f32_16x16x64_bf16 v[140:143], v[92:95], v[64:71], v12\n\ts_waitcnt vmcnt(6)\n\tv_mov_b64 v[250:251], 0\n\tv_smfmac_f32_16x16x64_bf16 v[124:127], v[96:99], a[64:71], v12\n\t" \
    "s_mov_b32 m0, s43\n\tv_max3_u32 v242, v96, v97, v242\n\tv_smfmac_f32_16x16x64_bf16 v[128:131], v[96:99], a[72:79], v12\n\tv_max3_u32 v242, v98, v99, v242\n\t" \
    "v_smfmac_f32_16x16x64_bf16 v[132:135], v[96:99], a[80:87], v12\n\tbuffer_load_dwordx4 v8, %[rs], s39 offen offset:0 sc1 lds\n\tv_smfmac_f32_16x16x64_bf16 v[136:139], v[96:99], a[88:95], v12\n\t" \
    "v_smfmac_f32_16x16x64_bf16 v[140:143], v[96:99], v[72:79], v12\n\ts_waitcnt vmcnt(6)\n\tv_cmp_eq_u32 vcc, -1, v242\n\tv_smfmac_f32_16x16x64_bf16 v[124:127], v[100:103], a[96:103], v12\n\t" \
    "v_max3_u32 v242, v100, v101, v242\n\tv_smfmac_f32_16x16x64_bf16 v[128:131], v[100:103], a[104:111], v12\n\tv_max3_u32 v242, v102, v103, v242\n\t" \
    "v_smfmac_f32_16x16x64_bf16 v[132:135], v[100:103], a[112:119], v12\n\tbuffer_load_dwordx4 v8, %[rs], s39 offen offset:1024 sc1 lds\n\t" \
    "v_smfmac_f32_16x16x64_bf16 v[136:139], v[100:103], a[120:127], v12\n\tv_smfmac_f32_16x16x64_bf16 v[140:143], v[100:103], v[80:87], v12\n\ts_waitcnt vmcnt(6)\n\ts_cbranch_vccnz 35f\n\t22:\n" \
    "v_smfmac_f32_16x16x64_bf16 v[124:127], v[104:107], a[128:135], v12\n\ts_mov_b32 m0, s44\n\tv_max3_u32 v242, v104, v105, v242\n\t" \
    "v_smfmac_f32_16x16x64_bf16 v[128:131], v[104:107], a[136:143], v12\n\tv_max3_u32 v242, v106, v107, v242\n\tv_smfmac_f32_16x16x64_bf16 v[132:135], v[104:107], a[144:151], v12\n\t" \
    "buffer_load_dwordx4 v9, %[rs], s40 offen offset:0 sc1 lds\n\tv_smfmac_f32_16x16x64_bf16 v[136:139], v[104:107], a[152:159], v12\n\ts_waitcnt vmcnt(6)\n\tv_mov_b64 v[252:253], 0\n\t" \
    "v_smfmac_f32_16x16x64_bf16 v[124:127], v[108:111], a[160:167], v12\n\ts_add_u32 m0, s43, 0x2000\n\tv_max3_u32 v242, v108, v109, v242\n\t" \
    "v_smfmac_f32_16x16x64_bf16 v[128:131], v[108:111], a[168:175], v12\n\tv_max3_u32 v242, v110, v111, v242\n\tv_smfmac_f32_16x16x64_bf16 v[132:135], v[108:111], a[176:183], v12\n\t" \
    "buffer_load_dwordx4 v8, %[rs], s41 offen offset:0 sc1 lds\n\tv_smfmac_f32_16x16x64_bf16 v[136:139], v[108:111], a[184:191], v12\n\ts_waitcnt vmcnt(6)\n\tv_mov_b64 v[254:255], 0\n\t" \
    "v_smfmac_f32_16x16x64_bf16 v[124:127], v[112:115], a[192:199], v12\n\tv_max3_u32 v242, v112, v113, v242\n\tv_smfmac_f32_16x16x64_bf16 v[128:131], v[112:115], a[200:207], v12\n\t" \
    "v_max3_u32 v242, v114, v115, v242\n\tv_smfmac_f32_16x16x64_bf16 v[132:135], v[112:115], a[208:215], v12\n\tbuffer_load_dwordx4 v8, %[rs], s41 offen offset:1024 sc1 lds\n\t" \
    "v_smfmac_f32_16x16x64_bf16 v[136:139], v[112:115], a[216:223], v12\n\ts_waitcnt vmcnt(6)\n\tv_mov_b32 v3, s49\n\tv_smfmac_f32_16x16x64_bf16 v[124:127], v[116:119], a[224:231], v12\n\t" \
    "v_max3_u32 v242, v116, v117, v242\n\tv_smfmac_f32_16x16x64_bf16 v[128:131], v[116:119], a[232:239], v12\n\tv_max3_u32 v242, v118, v119, v242\n\t" \
    "v_smfmac_f32_16x16x64_bf16 v[132:135], v[116:119], a[240:247], v12\n\tbuffer_store_dword v3, v13, %[rs], 0 offen offset:0 sc1\n\t" \
    "v_smfmac_f32_16x16x64_bf16 v[136:139], v[116:119], a[248:255], v12\n\ts_waitcnt vmcnt(6)\n\tv_smfmac_f32_16x16x64_bf16 v[124:127], v[120:123], v[24:31], v12\n\t" \
    "v_max3_u32 v242, v120, v121, v242\n\tv_smfmac_f32_16x16x64_bf16 v[128:131], v[120:123], v[32:39], v12\n\ts_waitcnt lgkmcnt(0)\n\tv_max3_u32 v242, v122, v123, v242\n\t" \
    "v_smfmac_f32_16x16x64_bf16 v[132:135], v[120:123], v[40:47], v12\n\tv_cmp_eq_u32 vcc, -1, v242\n\tv_smfmac_f32_16x16x64_bf16 v[136:139], v[120:123], v[48:55], v12\n\t" \
    "v_smfmac_f32_16x16x64_bf16 v[140:143], v[104:107], v[192:199], v12\n\tv_permlane32_swap_b32 v124, v128\n\tv_permlane32_swap_b32 v125, v129\n\tv_permlane32_swap_b32 v126, v130\n\t" \
    "v_permlane32_swap_b32 v127, v131\n\tv_pk_add_f32 v[124:125], v[124:125], v[128:129]\n\tv_pk_add_f32 v[126:127], v[126:127], v[130:131]\n\t" \
    "v_pk_mul_f32 v[124:125], v[124:125], s[58:59] op_sel_hi:[1,0]\n\tv_pk_mul_f32 v[126:127], v[126:127], s[58:59] op_sel_hi:[1,0]\n\tv_cvt_pk_bf16_f32 v124, v124, v125\n\t" \
    "v_cvt_pk_bf16_f32 v125, v126, v127\n\tv_permlane32_swap_b32 v132, v136\n\tv_permlane32_swap_b32 v133, v137\n\tv_permlane32_swap_b32 v134, v138\n\tv_permlane32_swap_b32 v135, v139\n\t" \
    "v_pk_add_f32 v[132:133], v[132:133], v[136:137]\n\tv_pk_add_f32 v[134:135], v[134:135], v[138:139]\n\tv_cvt_pk_bf16_f32 v126, v132, v133\n\tv_cvt_pk_bf16_f32 v127, v134, v135\n\t" \
    "ds_write_b128 v15, v[124:127] offset:0\n\tv_smfmac_f32_16x16x64_bf16 v[140:143], v[108:111], v[200:207], v12\n\tv_smfmac_f32_16x16x64_bf16 v[140:143], v[112:115], v[208:215], v12\n\t" \
    "s_cbranch_vccnz 30f\n\t21:\nv_smfmac_f32_16x16x64_bf16 v[140:143], v[116:119], v[216:223], v12\n\tv_mov_b64 v[212:213], 0\n\tv_mov_b64 v[214:215], 0\n\ts_waitcnt lgkmcnt(0)\n\ts_barrier\n\t" \
    "s_memtime s[92:93]\n\tds_read_b64_tr_b16 v[124:125], v16 offset:0\n\tds_read_b64_tr_b16 v[126:127], v16 offset:256\n\tds_read_b64_tr_b16 v[128:129], v16 offset:512\n\t" \
    "ds_read_b64_tr_b16 v[130:131], v16 offset:768\n\tv_smfmac_f32_16x16x64_bf16 v[140:143], v[120:123], v[224:231], v12\n\tds_read_b64_tr_b16 v[132:133], v16 offset:1024\n\t" \
    "ds_read_b64_tr_b16 v[134:135], v16 offset:1280\n\tds_read_b64_tr_b16 v[136:137], v16 offset:1536\n\tds_read_b64_tr_b16 v[138:139], v16 offset:1792\n\t" \
    "ds_read_b64_tr_b16 v[104:105], v16 offset:8\n\ts_waitcnt lgkmcnt(5)\n\tv_mfma_f32_16x16x32_bf16 v[192:195], v[144:147], v[124:127], 0\n\ts_add_u32 s94, s92, s45\n\t" \
    "ds_read_b64_tr_b16 v[106:107], v16 offset:264\n\tds_read_b64_tr_b16 v[108:109], v16 offset:520\n\tv_mfma_f32_16x16x32_bf16 v[196:199], v[160:163], v[124:127], 0\n\ts_mul_hi_u32 s94, s94, s46\n\t" \
    "ds_read_b64_tr_b16 v[110:111], v16 offset:776\n\tds_read_b64_tr_b16 v[112:113], v16 offset:1032\n\tv_mfma_f32_16x16x32_bf16 v[192:195], v[148:151], v[128:131], v[192:195]\n\t" \
    "s_waitcnt lgkmcnt(5)\n\tv_cvt_pk_bf16_f32 v218, v140, v141\n\tv_cvt_pk_bf16_f32 v219, v142, v143\n\tv_mfma_f32_16x16x32_bf16 v[196:199], v[164:167], v[128:131], v[196:199]\n\t" \
    "s_add_u32 s94, s94, s47\n\tds_write_b64 v17, v[218:219] offset:0\n\tds_read_b64_tr_b16 v[114:115], v16 offset:1288\n\tv_mfma_f32_16x16x32_bf16 v[192:195], v[152:155], v[132:135], v[192:195]\n\t" \
    "s_min_u32 s94, s94, 0xfffffffe\n\tds_read_b64_tr_b16 v[116:117], v16 offset:1544\n\tds_read_b64_tr_b16 v[118:119], v16 offset:1800\n\t" \
    "v_mfma_f32_16x16x32_bf16 v[196:199], v[168:171], v[132:135], v[196:199]\n\tv_mov_b32 v4, s94\n\tbuffer_store_dword v4, v14, %[rs], s97 offen offset:0 sc1\n\t" \
    "v_mfma_f32_16x16x32_bf16 v[192:195], v[156:159], v[136:139], v[192:195]\n\tds_read_b128 v[88:91], v21 offset:0\n\tds_read_b128 v[92:95], v21 offset:1024\n\t" \
    "v_mfma_f32_16x16x32_bf16 v[196:199], v[172:175], v[136:139], v[196:199]\n\ts_waitcnt lgkmcnt(5)\n\tds_read_b128 v[96:99], v21 offset:2048\n\tv_mov_b64 v[240:241], 0\n\t" \
    "v_mfma_f32_16x16x32_bf16 v[200:203], v[104:107], v[124:127], 0\n\ts_cmp_eq_u32 s49, 63\n\ts_barrier\n\ts_cbranch_scc1 78f\n\tds_read_b128 v[100:103], v21 offset:3072\n\t" \
    "v_mov_b64 v[242:243], 0\n\tv_mfma_f32_16x16x32_bf16 v[200:203], v[108:111], v[128:131], v[200:203]\n\ts_waitcnt lgkmcnt(4)\n\tds_read_b128 v[204:207], v18 offset:0\n\t" \
    "ds_read_b128 v[224:227], v21 offset:32768\n\tv_mfma_f32_16x16x32_bf16 v[200:203], v[112:115], v[132:135], v[200:203]\n\tv_max3_f32 v220, v192, v193, v194\n\tv_max3_f32 v221, v195, v196, v197\n\t" \
    "v_mfma_f32_16x16x32_bf16 v[200:203], v[116:119], v[136:139], v[200:203]\n\tv_max3_f32 v220, v220, v198, v199\n\tv_max_f32 v220, v220, v221\n\tv_mov_b32 v221, v220\n\t" \
    "ds_read_b128 v[208:211], v18 offset:1024\n\tds_read_b128 v[228:231], v21 offset:33792\n\tv_permlane32_swap_b32 v220, v221\n\tv_max_f32 v220, v220, v221\n\t" \
    "ds_read_b128 v[104:107], v21 offset:16384\n\tv_max3_f32 v220, v220, v200, v201\n\tv_max3_f32 v220, v220, v202, v203\n\tv_mov_b32 v221, v220\n\tds_read_b128 v[108:111], v21 offset:17408\n\t" \
    "ds_read_b128 v[112:115], v21 offset:18432\n\tv_permlane16_swap_b32 v220, v221\n\tv_max_f32 v140, v220, v221\n\t" \
    "v_pk_add_f32 v[192:193], v[192:193], v[140:141] op_sel_hi:[1,0] neg_lo:[0,1] neg_hi:[0,1]\n\tv_pk_add_f32 v[194:195], v[194:195], v[140:141] op_sel_hi:[1,0] neg_lo:[0,1] neg_hi:[0,1]\n\t" \
    "v_pk_add_f32 v[196:197], v[196:197], v[140:141] op_sel_hi:[1,0] neg_lo:[0,1] neg_hi:[0,1]\n\tv_pk_add_f32 v[198:199], v[198:199], v[140:141] op_sel_hi:[1,0] neg_lo:[0,1] neg_hi:[0,1]\n\t" \
    "v_exp_f32 v192, v192\n\tv_exp_f32 v193, v193\n\tv_exp_f32 v194, v194\n\tv_exp_f32 v195, v195\n\tv_exp_f32 v196, v196\n\tv_cvt_pk_bf16_f32 v220, v192, v193\n\tv_exp_f32 v197, v197\n\t" \
    "v_cvt_pk_bf16_f32 v221, v194, v195\n\tv_exp_f32 v198, v198\n\tv_exp_f32 v199, v199\n\tv_cvt_pk_bf16_f32 v222, v196, v197\n\tv_cvt_pk_bf16_f32 v223, v198, v199\n\t" \
    "v_pk_add_f32 v[200:201], v[200:201], v[140:141] op_sel_hi:[1,0] neg_lo:[0,1] neg_hi:[0,1]\n\tv_pk_add_f32 v[202:203], v[202:203], v[140:141] op_sel_hi:[1,0] neg_lo:[0,1] neg_hi:[0,1]\n\t" \
    "v_mfma_f32_16x16x32_bf16 v[124:127], v[176:179], v[220:223], 0\n\tv_exp_f32 v200, v200\n\tv_mfma_f32_16x16x32_bf16 v[140:143], v[220:223], v[244:247], 0\n\tv_exp_f32 v201, v201\n\t" \
    "v_mfma_f32_16x16x32_bf16 v[128:131], v[180:183], v[220:223], 0\n\tv_exp_f32 v202, v202\n\tv_mfma_f32_16x16x32_bf16 v[132:135], v[184:187], v[220:223], 0\n\tv_exp_f32 v203, v203\n\t" \
    "v_cvt_pk_bf16_f32 v218, v200, v201\n\tv_cvt_pk_bf16_f32 v219, v202, v203\n\tv_mfma_f32_16x16x32_bf16 v[136:139], v[188:191], v[220:223], 0\n\ts_waitcnt lgkmcnt(4)\n\t" \
    "ds_read_b128 v[116:119], v21 offset:19456\n\tds_read_b128 v[232:235], v21 offset:34816\n\tv_mfma_f32_16x16x16_bf16 v[140:143], v[218:219], v[6:7], v[140:143]\n\t" \
    "ds_read_b128 v[236:239], v21 offset:35840\n\tv_mov_b64 v[120:121], 0\n\tv_mfma_f32_16x16x16_bf16 v[124:127], v[204:205], v[218:219], v[124:127]\n\tds_read_b128 v[144:147], v21 offset:49152\n\t" \
    "v_mov_b64 v[122:123], 0\n\tv_mfma_f32_16x16x16_bf16 v[128:131], v[206:207], v[218:219], v[128:131]\n\tds_read_b128 v[148:151], v21 offset:50176\n\tds_read_b128 v[152:155], v21 offset:51200\n\t" \
    "v_mfma_f32_16x16x16_bf16 v[132:135], v[208:209], v[218:219], v[132:135]\n\tv_rcp_f32 v140, v140\n\tv_mfma_f32_16x16x16_bf16 v[136:139], v[210:211], v[218:219], v[136:139]\n\t" \
    "s_waitcnt lgkmcnt(5)\n\tv_rcp_f32 v141, v141\n\tv_cvt_pk_bf16_f32 v192, v124, v125\n\tv_cvt_pk_bf16_f32 v193, v126, v127\n\tv_cvt_pk_bf16_f32 v194, v128, v129\n\t" \
    "v_cvt_pk_bf16_f32 v195, v130, v131\n\tv_cvt_pk_bf16_f32 v196, v132, v133\n\tv_cvt_pk_bf16_f32 v197, v134, v135\n\tv_smfmac_f32_16x16x64_bf16 v[248:251], v[192:195], v[88:95], v12\n\t" \
    "v_cvt_pk_bf16_f32 v198, v136, v137\n\tv_cvt_pk_bf16_f32 v199, v138, v139\n\tds_read_b128 v[156:159], v21 offset:52224\n\ts_nop 0\n\t" \
    "v_smfmac_f32_16x16x64_bf16 v[248:251], v[196:199], v[96:103], v12\n\tv_rcp_f32 v142, v142\n\tv_smfmac_f32_16x16x64_bf16 v[252:255], v[192:195], v[104:111], v12\n\tv_rcp_f32 v143, v143\n\t" \
    "v_smfmac_f32_16x16x64_bf16 v[252:255], v[196:199], v[112:119], v12\n\ts_waitcnt lgkmcnt(2)\n\tv_mul_f32 v248, v248, v140\n\tv_mul_f32 v249, v249, v141\n\tv_mul_f32 v250, v250, v142\n\t" \
    "v_mul_f32 v251, v251, v143\n\tv_cvt_pk_bf16_f32 v124, v248, v249\n\tv_cvt_pk_bf16_f32 v125, v250, v251\n\tbuffer_store_dwordx2 v[124:125], v11, %[rs], s52 offen offset:0 sc1\n\t" \
    "v_smfmac_f32_16x16x64_bf16 v[212:215], v[192:195], v[224:231], v12\n\tds_read_b128 v[160:163], v21 offset:57344\n\tv_smfmac_f32_16x16x64_bf16 v[212:215], v[196:199], v[232:239], v12\n\t" \
    "s_waitcnt lgkmcnt(1)\n\tv_mul_f32 v252, v252, v140\n\tv_mul_f32 v253, v253, v141\n\tv_mul_f32 v254, v254, v142\n\tv_mul_f32 v255, v255, v143\n\tv_cvt_pk_bf16_f32 v126, v252, v253\n\t" \
    "v_cvt_pk_bf16_f32 v127, v254, v255\n\tbuffer_store_dwordx2 v[126:127], v11, %[rs], s53 offen offset:0 sc1\n\tv_smfmac_f32_16x16x64_bf16 v[240:243], v[192:195], v[144:151], v12\n\t" \
    "buffer_load_dword v4, v5, %[rs], s89 offen sc1\n\tv_smfmac_f32_16x16x64_bf16 v[240:243], v[196:199], v[152:159], v12\n\ts_cmp_eq_u32 s48, 0\n\tv_mul_f32 v212, v212, v140\n\t" \
    "v_mul_f32 v213, v213, v141\n\tv_mul_f32 v214, v214, v142\n\tv_mul_f32 v215, v215, v143\n\tv_cvt_pk_bf16_f32 v128, v212, v213\n\tv_cvt_pk_bf16_f32 v129, v214, v215\n\t" \
    "buffer_store_dwordx2 v[128:129], v11, %[rs], s54 offen offset:0 sc1\n\tds_read_b128 v[164:167], v21 offset:58368\n\tds_read_b128 v[168:171], v21 offset:59392\n\t" \
    "ds_read_b128 v[172:175], v21 offset:60416\n\tv_mul_f32 v240, v240, v140\n\tv_mul_f32 v241, v241, v141\n\tv_mul_f32 v242, v242, v142\n\tv_mul_f32 v243, v243, v143\n\t" \
    "v_cvt_pk_bf16_f32 v130, v240, v241\n\tv_cvt_pk_bf16_f32 v131, v242, v243\n\tbuffer_store_dwordx2 v[130:131], v11, %[rs], s55 offen offset:0 sc1\n\ts_memtime s[90:91]\n\ts_cbranch_scc1 40f\n\t" \
    "s_waitcnt lgkmcnt(0)\n\tv_smfmac_f32_16x16x64_bf16 v[120:123], v[192:195], v[160:167], v12\n\tv_smfmac_f32_16x16x64_bf16 v[120:123], v[196:199], v[168:175], v12\n\t79:\n" \
    "v_add_u32 v134, s36, v8\n\tv_add_u32 v135, s48, v134\n\tds_read_b128 v[248:251], v23 offset:0\n\tds_read_b128 v[252:255], v22 offset:0\n\tds_read_b128 v[88:91], v135 offset:0\n\t" \
    "ds_read_b128 v[144:147], v134 offset:0\n\tds_read_b128 v[160:163], v134 offset:4096\n\tds_read_b128 v[92:95], v135 offset:1024\n\tds_read_b128 v[148:151], v134 offset:1024\n\t" \
    "ds_read_b128 v[164:167], v134 offset:5120\n\tv_mul_f32 v120, v120, v140\n\tv_mul_f32 v121, v121, v141\n\tv_mul_f32 v122, v122, v142\n\tv_mul_f32 v123, v123, v143\n\t" \
    "v_cvt_pk_bf16_f32 v132, v120, v121\n\tv_cvt_pk_bf16_f32 v133, v122, v123\n\tbuffer_store_dwordx2 v[132:133], v11, %[rs], s56 offen offset:0 sc1\n\tds_read_b128 v[96:99], v135 offset:2048\n\t" \
    "ds_read_b128 v[152:155], v134 offset:2048\n\tds_read_b128 v[168:171], v134 offset:6144\n\tds_read_b128 v[100:103], v135 offset:3072\n\tds_read_b128 v[156:159], v134 offset:3072\n\t" \
    "ds_read_b128 v[172:175], v134 offset:7168\n\ts_waitcnt lgkmcnt(9)\n\tv_mfma_f32_16x16x32_bf16 v[192:195], v[144:147], v[88:91], v[248:251]\n\tds_read_b64_tr_b16 v[104:105], v16 offset:8\n\t" \
    "ds_read_b64_tr_b16 v[106:107], v16 offset:264\n\tv_mfma_f32_16x16x32_bf16 v[196:199], v[160:163], v[88:91], v[252:255]\n\ts_waitcnt lgkmcnt(8)\n\tds_read_b64_tr_b16 v[108:109], v16 offset:520\n\t" \
    "ds_read_b64_tr_b16 v[110:111], v16 offset:776\n\tv_mfma_f32_16x16x32_bf16 v[192:195], v[148:151], v[92:95], v[192:195]\n\tds_read_b64_tr_b16 v[112:113], v16 offset:1032\n\t" \
    "ds_read_b64_tr_b16 v[114:115], v16 offset:1288\n\tv_mfma_f32_16x16x32_bf16 v[196:199], v[164:167], v[92:95], v[196:199]\n\ts_waitcnt lgkmcnt(9)\n\t" \
    "ds_read_b64_tr_b16 v[116:117], v16 offset:1544\n\tds_read_b64_tr_b16 v[118:119], v16 offset:1800\n\tv_mfma_f32_16x16x32_bf16 v[192:195], v[152:155], v[96:99], v[192:195]\n\t" \
    "v_mfma_f32_16x16x32_bf16 v[196:199], v[168:171], v[96:99], v[196:199]\n\ts_waitcnt lgkmcnt(8)\n\tv_mfma_f32_16x16x32_bf16 v[192:195], v[156:159], v[100:103], v[192:195]\n\t" \
    "v_mfma_f32_16x16x32_bf16 v[196:199], v[172:175], v[100:103], v[196:199]\n\ts_waitcnt lgkmcnt(0)\n\tv_mfma_f32_16x16x32_bf16 v[200:203], v[104:107], v[88:91], 0\n\t" \
    "v_mfma_f32_16x16x32_bf16 v[200:203], v[108:111], v[92:95], v[200:203]\n\tv_mfma_f32_16x16x32_bf16 v[200:203], v[112:115], v[96:99], v[200:203]\n\tv_max3_f32 v220, v192, v193, v194\n\t" \
    "v_max3_f32 v221, v195, v196, v197\n\tv_mfma_f32_16x16x32_bf16 v[200:203], v[116:119], v[100:103], v[200:203]\n\tv_max3_f32 v220, v220, v198, v199\n\tv_max_f32 v220, v220, v221\n\t" \
    "v_mov_b32 v221, v220\n\ts_nop 0\n\ts_nop 0\n\tv_permlane32_swap_b32 v220, v221\n\tv_max_f32 v220, v220, v221\n\ts_nop 0\n\tv_max3_f32 v220, v220, v200, v201\n\t" \
    "v_max3_f32 v220, v220, v202, v203\n\tv_mov_b32 v221, v220\n\ts_nop 0\n\ts_nop 0\n\tv_permlane16_swap_b32 v220, v221\n\tv_max_f32 v140, v220, v221\n\t" \
    "v_pk_add_f32 v[192:193], v[192:193], v[140:141] op_sel_hi:[1,0] neg_lo:[0,1] neg_hi:[0,1]\n\tv_pk_add_f32 v[194:195], v[194:195], v[140:141] op_sel_hi:[1,0] neg_lo:[0,1] neg_hi:[0,1]\n\t" \
    "v_pk_add_f32 v[196:197], v[196:197], v[140:141] op_sel_hi:[1,0] neg_lo:[0,1] neg_hi:[0,1]\n\tv_pk_add_f32 v[198:199], v[198:199], v[140:141] op_sel_hi:[1,0] neg_lo:[0,1] neg_hi:[0,1]\n\t" \
    "v_exp_f32 v192, v192\n\tv_exp_f32 v193, v193\n\tv_exp_f32 v194, v194\n\tv_exp_f32 v195, v195\n\tv_exp_f32 v196, v196\n\tv_cvt_pk_bf16_f32 v220, v192, v193\n\tv_exp_f32 v197, v197\n\t" \
    "v_cvt_pk_bf16_f32 v221, v194, v195\n\tv_exp_f32 v198, v198\n\tv_exp_f32 v199, v199\n\tv_cvt_pk_bf16_f32 v222, v196, v197\n\tv_cvt_pk_bf16_f32 v223, v198, v199\n\t" \
    "v_pk_add_f32 v[200:201], v[200:201], v[140:141] op_sel_hi:[1,0] neg_lo:[0,1] neg_hi:[0,1]\n\tv_pk_add_f32 v[202:203], v[202:203], v[140:141] op_sel_hi:[1,0] neg_lo:[0,1] neg_hi:[0,1]\n\t" \
    "v_mfma_f32_16x16x32_bf16 v[124:127], v[176:179], v[220:223], 0\n\tv_exp_f32 v200, v200\n\tv_mfma_f32_16x16x32_bf16 v[140:143], v[244:247], v[220:223], 0\n\tv_exp_f32 v201, v201\n\t" \
    "v_mfma_f32_16x16x32_bf16 v[128:131], v[180:183], v[220:223], 0\n\tv_exp_f32 v202, v202\n\tv_mfma_f32_16x16x32_bf16 v[132:135], v[184:187], v[220:223], 0\n\tv_exp_f32 v203, v203\n\t" \
    "v_cvt_pk_bf16_f32 v218, v200, v201\n\tv_cvt_pk_bf16_f32 v219, v202, v203\n\tv_mfma_f32_16x16x32_bf16 v[136:139], v[188:191], v[220:223], 0\n\ts_nop 0\n\t" \
    "v_mfma_f32_16x16x16_bf16 v[140:143], v[6:7], v[218:219], v[140:143]\n\ts_waitcnt vmcnt(3)\n\tv_mfma_f32_16x16x16_bf16 v[124:127], v[204:205], v[218:219], v[124:127]\n\t" \
    "v_mfma_f32_16x16x16_bf16 v[128:131], v[206:207], v[218:219], v[128:131]\n\tv_mfma_f32_16x16x16_bf16 v[132:135], v[208:209], v[218:219], v[132:135]\n\tv_rcp_f32 v140, v140\n\t" \
    "v_mfma_f32_16x16x16_bf16 v[136:139], v[210:211], v[218:219], v[136:139]\n\tv_pk_mul_f32 v[124:125], v[124:125], v[140:141] op_sel_hi:[1,0]\n\t" \
    "v_pk_mul_f32 v[126:127], v[126:127], v[140:141] op_sel_hi:[1,0]\n\tv_cvt_pk_bf16_f32 v232, v124, v125\n\tv_cvt_pk_bf16_f32 v233, v126, v127\n\t" \
    "v_pk_mul_f32 v[128:129], v[128:129], v[140:141] op_sel_hi:[1,0]\n\tv_pk_mul_f32 v[130:131], v[130:131], v[140:141] op_sel_hi:[1,0]\n\tv_cvt_pk_bf16_f32 v234, v128, v129\n\t" \
    "v_cvt_pk_bf16_f32 v235, v130, v131\n\tbuffer_store_dwordx4 v[232:235], v8, %[rs], s42 offen offset:0 sc1\n\tv_pk_mul_f32 v[132:133], v[132:133], v[140:141] op_sel_hi:[1,0]\n\t" \
    "v_pk_mul_f32 v[134:135], v[134:135], v[140:141] op_sel_hi:[1,0]\n\tv_cvt_pk_bf16_f32 v236, v132, v133\n\tv_cvt_pk_bf16_f32 v237, v134, v135\n\t" \
    "v_pk_mul_f32 v[136:137], v[136:137], v[140:141] op_sel_hi:[1,0]\n\tv_pk_mul_f32 v[138:139], v[138:139], v[140:141] op_sel_hi:[1,0]\n\tv_cvt_pk_bf16_f32 v238, v136, v137\n\t" \
    "v_cvt_pk_bf16_f32 v239, v138, v139\n\tbuffer_store_dwordx4 v[236:239], v8, %[rs], s42 offen offset:1024 sc1\n\ts_branch 50f\n\t40:\ns_cmp_lg_u32 s75, 0\n\ts_cbranch_scc1 45f\n\t" \
    "s_memrealtime s[8:9]\n\ts_memtime s[10:11]\n\ts_nop 2\n\ts_memrealtime s[12:13]\n\ts_memtime s[14:15]\n\ts_nop 2\n\ts_memrealtime s[16:17]\n\ts_memtime s[18:19]\n\ts_nop 2\n\t" \
    "s_memrealtime s[20:21]\n\ts_memtime s[22:23]\n\ts_nop 2\n\ts_memrealtime s[24:25]\n\ts_memtime s[26:27]\n\ts_nop 2\n\ts_memrealtime s[28:29]\n\ts_memtime s[30:31]\n\ts_nop 2\n\t" \
    "s_memrealtime s[32:33]\n\ts_memtime s[34:35]\n\ts_nop 2\n\ts_waitcnt lgkmcnt(0)\n\ts_mov_b32 s94, s47\n\ts_mov_b32 s95, s50\n\ts_cmp_lg_u32 s32, s8\n\ts_cselect_b32 s94, s32, s94\n\t" \
    "s_cselect_b32 s95, s34, s95\n\ts_cmp_lg_u32 s28, s8\n\ts_cselect_b32 s94, s28, s94\n\ts_cselect_b32 s95, s30, s95\n\ts_cmp_lg_u32 s24, s8\n\ts_cselect_b32 s94, s24, s94\n\t" \
    "s_cselect_b32 s95, s26, s95\n\ts_cmp_lg_u32 s20, s8\n\ts_cselect_b32 s94, s20, s94\n\ts_cselect_b32 s95, s22, s95\n\ts_cmp_lg_u32 s16, s8\n\ts_cselect_b32 s94, s16, s94\n\t" \
    "s_cselect_b32 s95, s18, s95\n\ts_cmp_lg_u32 s12, s8\n\ts_cselect_b32 s94, s12, s94\n\ts_cselect_b32 s95, s14, s95\n\ts_sub_u32 s96, s94, s47\n\ts_sub_u32 s97, s95, s50\n\t" \
    "s_mul_i32 s98, s96, s51\n\ts_lshr_b32 s98, s98, 8\n\ts_sub_u32 s98, s97, s98\n\ts_ashr_i32 s98, s98, 1\n\ts_cmp_lt_u32 s96, 400\n\ts_cselect_b32 s98, s98, 0\n\ts_add_u32 s51, s51, s98\n\t" \
    "s_mov_b32 s47, s94\n\ts_mov_b32 s50, s95\n\tv_cvt_f32_u32 v3, s51\n\ts_sub_u32 s90, s90, s92\n\tv_rcp_f32 v3, v3\n\ts_add_u32 s63, s63, 8\n\ts_min_u32 s63, s63, s90\n\t" \
    "v_mul_f32 v3, 0x53800000, v3\n\tv_cvt_u32_f32 v3, v3\n\ts_sub_u32 s45, s63, s50\n\tv_readfirstlane_b32 s46, v3\n\ts_waitcnt vmcnt(2)\n\tv_cmp_eq_u32 vcc, -1, v4\n\ts_cbranch_vccnz 60f\n\t42:\n" \
    "v_subrev_u32 v4, s47, v4\n\ts_nop 1\n\tv_max_i32_dpp v4, v4, v4 row_ror:8 row_mask:0xf bank_mask:0xf\n\ts_nop 1\n\tv_max_i32_dpp v4, v4, v4 row_ror:4 row_mask:0xf bank_mask:0xf\n\ts_nop 1\n\t" \
    "v_max_i32_dpp v4, v4, v4 row_ror:2 row_mask:0xf bank_mask:0xf\n\ts_nop 1\n\tv_max_i32_dpp v4, v4, v4 row_ror:1 row_mask:0xf bank_mask:0xf\n\ts_nop 1\n\tv_readlane_b32 s94, v4, 0\n\t" \
    "v_readlane_b32 s95, v4, 16\n\tv_readlane_b32 s96, v4, 32\n\tv_readlane_b32 s97, v4, 48\n\ts_max_i32 s94, s94, s95\n\ts_max_i32 s96, s96, s97\n\ts_max_i32 s94, s94, s96\n\t" \
    "s_add_u32 s91, s94, s47\n\ts_lshr_b32 s95, s73, 2\n\ts_add_u32 s95, s95, s72\n\ts_sub_u32 s96, s91, s95\n\ts_sub_u32 s97, s91, s72\n\ts_lshl_b32 s97, s97, 2\n\ts_add_u32 s98, s73, s96\n\t" \
    "s_cmp_lt_i32 s96, -8\n\ts_cselect_b32 s98, s97, s98\n\ts_add_u32 s99, s73, 4\n\ts_add_u32 s93, s91, s79\n\ts_cmp_gt_i32 s96, 30\n\ts_cselect_b32 s92, s84, 0\n\ts_cselect_b32 s84, 0, s85\n\t" \
    "s_cmp_lt_u32 s49, 8\n\ts_cselect_b32 s92, 0, s92\n\ts_cmp_eq_u32 s49, 8\n\ts_cselect_b32 s92, -6, s92\n\ts_add_u32 s77, s77, s92\n\ts_ashr_i32 s92, s92, 1\n\ts_add_u32 s59, s59, s92\n\t" \
    "s_add_u32 s77, s77, s92\n\ts_cmp_gt_i32 s96, s78\n\ts_cselect_b32 s98, s99, s98\n\ts_cselect_b32 s95, s93, s95\n\ts_min_u32 s97, s97, s73\n\ts_add_u32 s93, s93, 20\n\ts_cmp_lt_u32 s49, 2\n\t" \
    "s_cselect_b32 s73, s97, s98\n\ts_cselect_b32 s95, s93, s95\n\ts_add_u32 s72, s95, s77\n\ts_sub_u32 s96, s72, s47\n\ts_mul_i32 s96, s96, s51\n\ts_ashr_i32 s96, s96, 8\n\t" \
    "s_add_u32 s96, s96, s50\n\ts_or_b32 s38, s96, 1\n\ts_branch 50f\n\t45:\ns_waitcnt vmcnt(2)\n\ts_add_u32 s94, s49, 3\n\ts_min_u32 s94, s94, 64\n\ts_cmp_lt_u32 s57, s94\n\ts_cbranch_scc1 70f\n\t" \
    "50:\ns_cmp_eq_u32 s49, 0\n\ts_cbranch_scc1 75f\n\t51:\ns_add_u32 s37, s37, 0x8000\n\ts_lshl_b32 s94, s75, 1\n\ts_add_u32 s43, s36, s94\n\ts_add_u32 s44, s36, s75\n\ts_add_u32 s44, s44, 0x4000\n\t" \
    "s_sub_u32 s36, s74, s36\n\ts_add_u32 s94, s49, 2\n\ts_min_u32 s94, s94, 63\n\ts_and_b32 s96, s94, 15\n\ts_lshr_b32 s97, s83, s96\n\ts_and_b32 s97, s97, 1\n\ts_lshl_b32 s97, s97, 13\n\t" \
    "s_lshl_b32 s96, s96, 21\n\ts_or_b32 s94, s96, s97\n\ts_add_u32 s39, s80, s94\n\ts_add_u32 s40, s81, s94\n\ts_add_u32 s41, s82, s94\n\ts_add_u32 s94, s49, 1\n\ts_and_b32 s96, s94, 15\n\t" \
    "s_lshr_b32 s97, s83, s96\n\ts_and_b32 s97, s97, 1\n\ts_lshl_b32 s97, s97, 13\n\ts_lshl_b32 s96, s96, 21\n\ts_or_b32 s94, s96, s97\n\ts_add_u32 s42, s76, s94\n\ts_cmp_lt_u32 s49, 62\n\t" \
    "s_cselect_b32 s95, s70, 0x40000000\n\ts_add_u32 s52, s95, s64\n\ts_add_u32 s53, s95, s65\n\ts_add_u32 s54, s95, s66\n\ts_add_u32 s55, s95, s67\n\ts_add_u32 s56, s95, s68\n\ts_mov_b32 s94, s69\n\t" \
    "s_mov_b32 s69, s70\n\ts_mov_b32 s70, s71\n\ts_mov_b32 s71, s94\n\ts_add_u32 s49, s49, 1\n\ts_cmp_lt_u32 s49, 64\n\ts_cbranch_scc1 1b\n\ts_waitcnt lgkmcnt(0)\n\ts_nop 7\n\t.subsection 1\n\t30:\n" \
    "s_add_u32 s61, s61, 1\n\ts_add_u32 s87, s87, 1\n\ts_cmp_lt_u32 s87, s60\n\ts_cbranch_scc0 33f\n\ts_waitcnt lgkmcnt(0)\n\ts_nop 7\n\tv_mov_b32 v242, 0\n\tv_mov_b64 v[124:125], 0\n\t" \
    "v_mov_b64 v[126:127], 0\n\tv_mov_b64 v[128:129], 0\n\tv_mov_b64 v[130:131], 0\n\tv_mov_b64 v[132:133], 0\n\tv_mov_b64 v[134:135], 0\n\tv_mov_b64 v[136:137], 0\n\tv_mov_b64 v[138:139], 0\n\t" \
    "v_mov_b64 v[140:141], 0\n\tv_mov_b64 v[142:143], 0\n\ts_branch 15b\n\t33:\ns_mov_b32 s60, 0\n\ts_branch 21b\n\t35:\ns_cmp_eq_u32 s60, 0\n\ts_cbranch_scc1 22b\n\ts_branch 30b\n\t60:\n" \
    "s_mov_b32 s95, 0\n\ts_add_u32 s97, s37, 0x4000\n\t61:\nbuffer_load_dword v4, v5, %[rs], s97 offen sc1\n\ts_add_u32 s95, s95, 1\n\ts_add_u32 s62, s62, 1\n\ts_waitcnt vmcnt(0)\n\t" \
    "v_cmp_eq_u32 vcc, -1, v4\n\ts_cbranch_vccz 42b\n\ts_cmp_lt_u32 s95, s60\n\ts_cbranch_scc1 61b\n\ts_mov_b32 s60, 0\n\ts_branch 42b\n\t75:\ns_barrier\n\ts_and_b32 s96, 1, 15\n\t" \
    "s_lshr_b32 s97, s83, s96\n\ts_and_b32 s97, s97, 1\n\ts_lshl_b32 s97, s97, 13\n\ts_lshl_b32 s96, s96, 21\n\ts_or_b32 s94, s96, s97\n\ts_add_u32 s95, s80, s94\n\ts_mov_b32 m0, s43\n\ts_nop 0\n\t" \
    "buffer_load_dwordx4 v8, %[rs], s95 offen offset:0 sc1 lds\n\tbuffer_load_dwordx4 v8, %[rs], s95 offen offset:1024 sc1 lds\n\ts_add_u32 s95, s81, s94\n\ts_mov_b32 m0, s44\n\ts_nop 0\n\t" \
    "buffer_load_dwordx4 v9, %[rs], s95 offen offset:0 sc1 lds\n\ts_add_u32 s95, s82, s94\n\ts_add_u32 m0, s43, 0x2000\n\ts_nop 0\n\tbuffer_load_dwordx4 v8, %[rs], s95 offen offset:0 sc1 lds\n\t" \
    "buffer_load_dwordx4 v8, %[rs], s95 offen offset:1024 sc1 lds\n\ts_waitcnt vmcnt(0)\n\ts_branch 51b\n\t78:\nds_read_b128 v[204:207], v18\n\tds_read_b128 v[208:211], v18 offset:1024\n\t" \
    "s_waitcnt lgkmcnt(0)\n\ts_cmp_eq_u32 s48, 0\n\ts_cbranch_scc0 79b\n\ts_branch 50b\n\t70:\ns_mov_b32 s95, 0\n\t71:\nbuffer_load_dword v4, v5, %[rs], 0 offen sc1\n\ts_add_u32 s95, s95, 1\n\t" \
    "s_waitcnt vmcnt(0)\n\tv_readfirstlane_b32 s57, v4\n\ts_nop 1\n\ts_cmp_ge_u32 s57, s94\n\ts_cbranch_scc1 50b\n\ts_sleep 1\n\ts_cmp_lt_u32 s95, s60\n\ts_cbranch_scc1 71b\n\ts_mov_b32 s60, 0\n\t" \
    "s_branch 50b\n\t.subsection 0\n\t"
#define RED_LOOP \
    "1:\ns_add_u32 s52, s69, s64\n\ts_add_u32 s53, s52, 0x1000\n\ts_add_u32 s54, s37, s65\n\ts_add_u32 s89, s37, 0x4000\n\ts_sub_u32 s92, s43, s74\n\ts_mov_b32 s95, 0\n\t14:\ns_memtime s[98:99]\n\t" \
    "s_add_u32 s95, s95, 1\n\ts_cmp_gt_u32 s95, 400\n\ts_cbranch_scc1 16f\n\ts_waitcnt lgkmcnt(0)\n\ts_sub_u32 s94, s92, s98\n\ts_cmp_gt_i32 s94, 170\n\ts_cbranch_scc1 14b\n\t" \
    "s_sub_u32 s94, s94, 60\n\ts_cmp_gt_i32 s94, 0\n\ts_cbranch_scc0 16f\n\t15:\ns_sub_u32 s94, s94, 24\n\ts_cbranch_scc0 15b\n\t16:\ns_waitcnt lgkmcnt(0)\n\ts_mov_b64 exec, 63\n\t" \
    "buffer_load_dword v53, v3, %[rs], s89 offen sc1\n\ts_mov_b64 exec, -1\n\ts_memrealtime s[8:9]\n\ts_memtime s[10:11]\n\ts_nop 2\n\ts_memrealtime s[12:13]\n\ts_memtime s[14:15]\n\ts_nop 2\n\t" \
    "s_memrealtime s[16:17]\n\ts_memtime s[18:19]\n\ts_nop 2\n\ts_memrealtime s[20:21]\n\ts_memtime s[22:23]\n\ts_nop 2\n\ts_memrealtime s[24:25]\n\ts_memtime s[26:27]\n\ts_nop 2\n\t" \
    "s_memrealtime s[28:29]\n\ts_memtime s[30:31]\n\ts_nop 2\n\ts_memrealtime s[32:33]\n\ts_memtime s[34:35]\n\ts_nop 2\n\ts_waitcnt lgkmcnt(0)\n\ts_mov_b32 s94, s47\n\ts_mov_b32 s95, s50\n\t" \
    "s_cmp_lg_u32 s32, s8\n\ts_cselect_b32 s94, s32, s94\n\ts_cselect_b32 s95, s34, s95\n\ts_cmp_lg_u32 s28, s8\n\ts_cselect_b32 s94, s28, s94\n\ts_cselect_b32 s95, s30, s95\n\t" \
    "s_cmp_lg_u32 s24, s8\n\ts_cselect_b32 s94, s24, s94\n\ts_cselect_b32 s95, s26, s95\n\ts_cmp_lg_u32 s20, s8\n\ts_cselect_b32 s94, s20, s94\n\ts_cselect_b32 s95, s22, s95\n\t" \
    "s_cmp_lg_u32 s16, s8\n\ts_cselect_b32 s94, s16, s94\n\ts_cselect_b32 s95, s18, s95\n\ts_cmp_lg_u32 s12, s8\n\ts_cselect_b32 s94, s12, s94\n\ts_cselect_b32 s95, s14, s95\n\t" \
    "s_sub_u32 s96, s94, s47\n\ts_sub_u32 s97, s95, s50\n\ts_mul_i32 s98, s96, s51\n\ts_lshr_b32 s98, s98, 8\n\ts_sub_u32 s98, s97, s98\n\ts_ashr_i32 s98, s98, 1\n\ts_cmp_lt_u32 s96, 400\n\t" \
    "s_cselect_b32 s98, s98, 0\n\ts_add_u32 s51, s51, s98\n\ts_mov_b32 s47, s94\n\ts_mov_b32 s50, s95\n\ts_sub_u32 s43, s42, s47\n\ts_mul_i32 s43, s43, s51\n\ts_ashr_i32 s43, s43, 8\n\t" \
    "s_add_u32 s43, s43, s50\n\ts_sub_u32 s93, s42, s59\n\ts_add_u32 s93, s93, s78\n\ts_waitcnt vmcnt(0)\n\tv_cmp_eq_u32 s[96:97], -1, v53\n\tv_subrev_u32 v53, s93, v53\n\tv_cmp_lt_i32 vcc, 0, v53\n\t" \
    "s_or_b32 s94, vcc_lo, s96\n\ts_lshr_b32 s96, s94, 1\n\ts_and_b32 s94, s94, s96\n\ts_and_b32 s94, s94, s57\n\ts_cmp_lt_u32 s49, 2\n\ts_cselect_b32 s94, 1, s94\n\ts_cmp_lg_u32 s94, 0\n\t" \
    "s_cbranch_scc1 66f\n\t45:\ns_mov_b32 s95, 0\n\t20:\ns_memtime s[98:99]\n\ts_memrealtime s[8:9]\n\ts_add_u32 s95, s95, 1\n\ts_cmp_gt_u32 s95, 400\n\ts_cbranch_scc1 22f\n\ts_waitcnt lgkmcnt(0)\n\t" \
    "s_sub_u32 s94, s43, s98\n\ts_sub_u32 s8, s42, s8\n\ts_mul_i32 s8, s8, s51\n\ts_ashr_i32 s8, s8, 8\n\ts_min_i32 s94, s94, s8\n\ts_sub_u32 s8, s8, 96\n\ts_max_i32 s94, s94, s8\n\t" \
    "s_cmp_gt_i32 s94, 194\n\ts_cbranch_scc1 20b\n\ts_sub_u32 s94, s94, 84\n\ts_cmp_gt_i32 s94, 0\n\ts_cbranch_scc0 22f\n\t21:\ns_sub_u32 s94, s94, 24\n\ts_cbranch_scc0 21b\n\t22:\n" \
    "s_waitcnt lgkmcnt(0)\n\tbuffer_load_dwordx4 v[16:19], v6, %[rs], s52 offen offset:0 sc1\n\tbuffer_load_dwordx4 v[20:23], v6, %[rs], s52 offen offset:1024 sc1\n\t" \
    "buffer_load_dwordx4 v[24:27], v6, %[rs], s52 offen offset:2048 sc1\n\tbuffer_load_dwordx4 v[28:31], v6, %[rs], s52 offen offset:3072 sc1\n\t" \
    "buffer_load_dwordx4 v[32:35], v6, %[rs], s53 offen offset:0 sc1\n\tbuffer_load_dwordx4 v[36:39], v6, %[rs], s53 offen offset:1024 sc1\n\t" \
    "buffer_load_dwordx4 v[40:43], v6, %[rs], s53 offen offset:2048 sc1\n\tbuffer_load_dwordx4 v[44:47], v6, %[rs], s53 offen offset:3072 sc1\n\ts_waitcnt vmcnt(7)\n\t" \
    "v_mfma_f32_16x16x32_bf16 v[48:51], v[16:19], v[8:11], 0\n\ts_waitcnt vmcnt(6)\n\tv_mfma_f32_16x16x32_bf16 v[48:51], v[20:23], v[8:11], v[48:51]\n\ts_waitcnt vmcnt(5)\n\t" \
    "v_mfma_f32_16x16x32_bf16 v[48:51], v[24:27], v[8:11], v[48:51]\n\ts_waitcnt vmcnt(4)\n\tv_mfma_f32_16x16x32_bf16 v[48:51], v[28:31], v[8:11], v[48:51]\n\ts_waitcnt vmcnt(3)\n\t" \
    "v_mfma_f32_16x16x32_bf16 v[48:51], v[32:35], v[8:11], v[48:51]\n\ts_waitcnt vmcnt(2)\n\tv_mfma_f32_16x16x32_bf16 v[48:51], v[36:39], v[8:11], v[48:51]\n\ts_waitcnt vmcnt(1)\n\t" \
    "v_mfma_f32_16x16x32_bf16 v[48:51], v[40:43], v[8:11], v[48:51]\n\ts_waitcnt vmcnt(0)\n\tv_mfma_f32_16x16x32_bf16 v[48:51], v[44:47], v[8:11], v[48:51]\n\ts_nop 7\n\t" \
    "v_permlane32_swap_b32 v48, v50\n\tv_permlane32_swap_b32 v49, v51\n\tv_pk_add_f32 v[48:49], v[48:49], v[50:51]\n\ts_nop 0\n\tv_cmp_u_f32 vcc, v48, v49\n\tv_cvt_pk_bf16_f32 v52, v48, v49\n\t" \
    "s_cbranch_vccnz 30f\n\t43:\nbuffer_store_dword v52, v7, %[rs], s54 offen sc1\n\tbuffer_load_dword v4, v5, %[rs], s89 offen sc1\n\ts_waitcnt vmcnt(0)\n\tv_cmp_eq_u32 vcc, -1, v4\n\t" \
    "s_cbranch_vccnz 60f\n\t42:\nv_subrev_u32 v4, s47, v4\n\ts_nop 1\n\tv_max_i32_dpp v4, v4, v4 row_ror:8 row_mask:0xf bank_mask:0xf\n\ts_nop 1\n\t" \
    "v_max_i32_dpp v4, v4, v4 row_ror:4 row_mask:0xf bank_mask:0xf\n\ts_nop 1\n\tv_max_i32_dpp v4, v4, v4 row_ror:2 row_mask:0xf bank_mask:0xf\n\ts_nop 1\n\t" \
    "v_max_i32_dpp v4, v4, v4 row_ror:1 row_mask:0xf bank_mask:0xf\n\ts_nop 1\n\tv_readlane_b32 s94, v4, 0\n\tv_readlane_b32 s95, v4, 16\n\tv_readlane_b32 s96, v4, 32\n\tv_readlane_b32 s97, v4, 48\n\t" \
    "s_max_i32 s94, s94, s95\n\ts_max_i32 s96, s96, s97\n\ts_max_i32 s94, s94, s96\n\ts_add_u32 s91, s94, s47\n\ts_lshr_b32 s95, s73, 2\n\ts_add_u32 s95, s95, s72\n\ts_sub_u32 s96, s91, s95\n\t" \
    "s_sub_u32 s97, s91, s72\n\ts_lshl_b32 s97, s97, 2\n\ts_add_u32 s98, s73, s96\n\ts_cmp_lt_i32 s96, -8\n\ts_cselect_b32 s98, s97, s98\n\ts_add_u32 s99, s73, 4\n\ts_add_u32 s93, s91, s79\n\t" \
    "s_cmp_gt_i32 s96, 30\n\ts_cselect_b32 s92, s84, 0\n\ts_cselect_b32 s84, 0, s85\n\ts_cmp_lt_u32 s49, 8\n\ts_cselect_b32 s92, 0, s92\n\ts_cmp_eq_u32 s49, 8\n\ts_cselect_b32 s92, -6, s92\n\t" \
    "s_add_u32 s77, s77, s92\n\ts_ashr_i32 s92, s92, 1\n\ts_add_u32 s59, s59, s92\n\ts_add_u32 s77, s77, s92\n\ts_cmp_gt_i32 s96, s78\n\ts_cselect_b32 s98, s99, s98\n\ts_cselect_b32 s95, s93, s95\n\t" \
    "s_min_u32 s97, s97, s73\n\ts_add_u32 s93, s93, 20\n\ts_cmp_lt_u32 s49, 2\n\ts_cselect_b32 s73, s97, s98\n\ts_cselect_b32 s95, s93, s95\n\ts_add_u32 s72, s95, s77\n\ts_lshr_b32 s93, s73, 2\n\t" \
    "s_add_u32 s93, s93, s72\n\ts_add_u32 s42, s93, s59\n\ts_sub_u32 s43, s42, s47\n\ts_mul_i32 s43, s43, s51\n\ts_ashr_i32 s43, s43, 8\n\ts_add_u32 s43, s43, s50\n\ts_add_u32 s93, s72, s75\n\t" \
    "s_sub_u32 s92, s93, s47\n\ts_mul_i32 s92, s92, s51\n\ts_ashr_i32 s92, s92, 8\n\ts_add_u32 s92, s92, s50\n\ts_mov_b32 s95, 0\n\t24:\ns_memtime s[98:99]\n\ts_add_u32 s95, s95, 1\n\t" \
    "s_cmp_gt_u32 s95, 400\n\ts_cbranch_scc1 26f\n\ts_waitcnt lgkmcnt(0)\n\ts_sub_u32 s94, s92, s98\n\ts_cmp_gt_i32 s94, 170\n\ts_cbranch_scc1 24b\n\ts_sub_u32 s94, s94, 60\n\ts_cmp_gt_i32 s94, 0\n\t" \
    "s_cbranch_scc0 26f\n\t25:\ns_sub_u32 s94, s94, 24\n\ts_cbranch_scc0 25b\n\t26:\ns_waitcnt lgkmcnt(0)\n\tbuffer_store_dwordx4 v[12:15], v6, %[rs], s52 offen offset:0 sc1\n\t" \
    "buffer_store_dwordx4 v[12:15], v6, %[rs], s52 offen offset:1024 sc1\n\tbuffer_store_dwordx4 v[12:15], v6, %[rs], s52 offen offset:2048 sc1\n\t" \
    "buffer_store_dwordx4 v[12:15], v6, %[rs], s52 offen offset:3072 sc1\n\tbuffer_store_dwordx4 v[12:15], v6, %[rs], s53 offen offset:0 sc1\n\t" \
    "buffer_store_dwordx4 v[12:15], v6, %[rs], s53 offen offset:1024 sc1\n\tbuffer_store_dwordx4 v[12:15], v6, %[rs], s53 offen offset:2048 sc1\n\t" \
    "buffer_store_dwordx4 v[12:15], v6, %[rs], s53 offen offset:3072 sc1\n\ts_add_u32 s92, s54, 0x8000\n\ts_cmp_lt_u32 s49, 62\n\ts_cselect_b32 s92, s92, 0x40000000\n\ts_mov_b64 exec, 0xff\n\t" \
    "buffer_store_dwordx4 v[12:15], v6, %[rs], s92 offen sc1\n\ts_mov_b64 exec, -1\n\ts_mov_b32 s94, s69\n\ts_mov_b32 s69, s70\n\ts_mov_b32 s70, s71\n\ts_mov_b32 s71, s94\n\t" \
    "s_add_u32 s37, s37, 0x8000\n\ts_add_u32 s49, s49, 1\n\ts_cmp_lt_u32 s49, 63\n\ts_cbranch_scc1 1b\n\t.subsection 1\n\t30:\ns_add_u32 s61, s61, 1\n\ts_mov_b32 s95, 0\n\t31:\n" \
    "buffer_load_dwordx4 v[16:19], v6, %[rs], s52 offen offset:0 sc1\n\tbuffer_load_dwordx4 v[20:23], v6, %[rs], s52 offen offset:1024 sc1\n\t" \
    "buffer_load_dwordx4 v[24:27], v6, %[rs], s52 offen offset:2048 sc1\n\tbuffer_load_dwordx4 v[28:31], v6, %[rs], s52 offen offset:3072 sc1\n\t" \
    "buffer_load_dwordx4 v[32:35], v6, %[rs], s53 offen offset:0 sc1\n\tbuffer_load_dwordx4 v[36:39], v6, %[rs], s53 offen offset:1024 sc1\n\t" \
    "buffer_load_dwordx4 v[40:43], v6, %[rs], s53 offen offset:2048 sc1\n\tbuffer_load_dwordx4 v[44:47], v6, %[rs], s53 offen offset:3072 sc1\n\ts_add_u32 s95, s95, 1\n\ts_waitcnt vmcnt(7)\n\t" \
    "v_mfma_f32_16x16x32_bf16 v[48:51], v[16:19], v[8:11], 0\n\ts_waitcnt vmcnt(6)\n\tv_mfma_f32_16x16x32_bf16 v[48:51], v[20:23], v[8:11], v[48:51]\n\ts_waitcnt vmcnt(5)\n\t" \
    "v_mfma_f32_16x16x32_bf16 v[48:51], v[24:27], v[8:11], v[48:51]\n\ts_waitcnt vmcnt(4)\n\tv_mfma_f32_16x16x32_bf16 v[48:51], v[28:31], v[8:11], v[48:51]\n\ts_waitcnt vmcnt(3)\n\t" \
    "v_mfma_f32_16x16x32_bf16 v[48:51], v[32:35], v[8:11], v[48:51]\n\ts_waitcnt vmcnt(2)\n\tv_mfma_f32_16x16x32_bf16 v[48:51], v[36:39], v[8:11], v[48:51]\n\ts_waitcnt vmcnt(1)\n\t" \
    "v_mfma_f32_16x16x32_bf16 v[48:51], v[40:43], v[8:11], v[48:51]\n\ts_waitcnt vmcnt(0)\n\tv_mfma_f32_16x16x32_bf16 v[48:51], v[44:47], v[8:11], v[48:51]\n\ts_nop 7\n\t" \
    "v_permlane32_swap_b32 v48, v50\n\tv_permlane32_swap_b32 v49, v51\n\tv_pk_add_f32 v[48:49], v[48:49], v[50:51]\n\ts_nop 0\n\tv_cmp_u_f32 vcc, v48, v49\n\tv_cvt_pk_bf16_f32 v52, v48, v49\n\t" \
    "s_cbranch_vccz 43b\n\ts_and_b32 s94, s95, 7\n\ts_cbranch_scc1 31b\n\tv_max_u32 v53, v16, v17\n\tv_max3_u32 v53, v18, v19, v53\n\tv_max3_u32 v53, v20, v21, v53\n\tv_max3_u32 v53, v22, v23, v53\n\t" \
    "v_max3_u32 v53, v24, v25, v53\n\tv_max3_u32 v53, v26, v27, v53\n\tv_max3_u32 v53, v28, v29, v53\n\tv_max3_u32 v53, v30, v31, v53\n\tv_max3_u32 v53, v32, v33, v53\n\t" \
    "v_max3_u32 v53, v34, v35, v53\n\tv_max3_u32 v53, v36, v37, v53\n\tv_max3_u32 v53, v38, v39, v53\n\tv_max3_u32 v53, v40, v41, v53\n\tv_max3_u32 v53, v42, v43, v53\n\t" \
    "v_max3_u32 v53, v44, v45, v53\n\tv_max3_u32 v53, v46, v47, v53\n\tv_cmp_eq_u32 vcc, -1, v53\n\tv_min_u32 v52, -2, v52\n\ts_cbranch_vccz 43b\n\ts_cmp_lt_u32 s95, s60\n\ts_cbranch_scc1 31b\n\t32:\n" \
    "s_mov_b32 s60, 0\n\ts_branch 43b\n\t60:\ns_mov_b32 s95, 0\n\ts_add_u32 s97, s37, 0x4000\n\t61:\nbuffer_load_dword v4, v5, %[rs], s97 offen sc1\n\ts_add_u32 s95, s95, 1\n\t" \
    "s_add_u32 s62, s62, 1\n\ts_waitcnt vmcnt(0)\n\tv_cmp_eq_u32 vcc, -1, v4\n\ts_cbranch_vccz 42b\n\ts_cmp_lt_u32 s95, s60\n\ts_cbranch_scc1 61b\n\ts_mov_b32 s60, 0\n\ts_branch 42b\n\t68:\n" \
    "v_subrev_u32 v4, s47, v4\n\ts_nop 1\n\tv_max_i32_dpp v4, v4, v4 row_ror:8 row_mask:0xf bank_mask:0xf\n\ts_nop 1\n\tv_max_i32_dpp v4, v4, v4 row_ror:4 row_mask:0xf bank_mask:0xf\n\ts_nop 1\n\t" \
    "v_max_i32_dpp v4, v4, v4 row_ror:2 row_mask:0xf bank_mask:0xf\n\ts_nop 1\n\tv_max_i32_dpp v4, v4, v4 row_ror:1 row_mask:0xf bank_mask:0xf\n\ts_nop 1\n\tv_readlane_b32 s94, v4, 0\n\t" \
    "v_readlane_b32 s95, v4, 16\n\tv_readlane_b32 s96, v4, 32\n\tv_readlane_b32 s97, v4, 48\n\ts_max_i32 s94, s94, s95\n\ts_max_i32 s96, s96, s97\n\ts_max_i32 s94, s94, s96\n\t" \
    "s_add_u32 s91, s94, s47\n\ts_add_u32 s92, s91, s59\n\ts_sub_u32 s94, s92, s42\n\ts_cmp_gt_i32 s94, 0\n\ts_cselect_b32 s42, s92, s42\n\ts_sub_u32 s43, s42, s47\n\ts_mul_i32 s43, s43, s51\n\t" \
    "s_ashr_i32 s43, s43, 8\n\ts_add_u32 s43, s43, s50\n\ts_branch 45b\n\t66:\ns_mov_b32 s95, 0\n\ts_add_u32 s97, s37, 0x4000\n\t67:\nbuffer_load_dword v4, v5, %[rs], s97 offen sc1\n\t" \
    "s_add_u32 s95, s95, 1\n\ts_add_u32 s62, s62, 1\n\ts_waitcnt vmcnt(0)\n\tv_cmp_eq_u32 vcc, -1, v4\n\ts_cbranch_vccz 68b\n\ts_cmp_lt_u32 s95, s60\n\ts_cbranch_scc1 67b\n\ts_mov_b32 s60, 0\n\t" \
    "s_branch 68b\n\t.subsection 0\n\t"
#define CLOB_S "s86", "s87", "s8", "s9", "s10", "s11", "s12", "s13", "s14", "s15", "s16", "s17", "s18", "s19", "s20", "s21", "s22", "s23", "s24", "s25", "s26", "s27", "s28", "s29", "s30", "s31", "s32", "s33", "s34", "s35", "s89", "s90", "s91", "s92", "s93", "s94", "s95", "s96", "s97", "s98", "s99"
#define CLOB_V "v88", "v89", "v90", "v91", "v92", "v93", "v94", "v95", "v96", "v97", "v98", "v99", "v100", "v101", "v102", "v103", "v104", "v105", "v106", "v107", "v108", "v109", "v110", "v111", "v112", "v113", "v114", "v115", "v116", "v117", "v118", "v119", "v120", "v121", "v122", "v123", "v124", "v125", "v126", "v127", "v128", "v129", "v130", "v131", "v132", "v133", "v134", "v135", "v136", "v137", "v138", "v139", "v140", "v141", "v142", "v143", "v144", "v145", "v146", "v147", "v148", "v149", "v150", "v151", "v152", "v153", "v154", "v155", "v156", "v157", "v158", "v159", "v160", "v161", "v162", "v163", "v164", "v165", "v166", "v167", "v168", "v169", "v170", "v171", "v172", "v173", "v174", "v175", "v176", "v177", "v178", "v179", "v180", "v181", "v182", "v183", "v184", "v185", "v186", "v187", "v188", "v189", "v190", "v191", "v192", "v193", "v194", "v195", "v196", "v197", "v198", "v199", "v200", "v201", "v202", "v203", "v204", "v205", "v206", "v207", "v208", "v209", "v210", "v211", "v212", "v213", "v214", "v215", "v216", "v217", "v218", "v219", "v220", "v221", "v222", "v223", "v224", "v225", "v226", "v227", "v228", "v229", "v230", "v231", "v240", "v241", "v242", "v243", "v248", "v249", "v250", "v251", "v252", "v253", "v254", "v255"
typedef unsigned u16v __attribute__((ext_vector_type(16)));
typedef unsigned u32v __attribute__((ext_vector_type(32)));
DEV u32v cat4(const u8v& a, const u8v& b, const u8v& c, const u8v& d) {
    const u16v x = __builtin_shufflevector(a, b, 0, 1, 2, 3, 4, 5, 6, 7, 8, 9, 10, 11, 12, 13, 14, 15), y = __builtin_shufflevector(c, d, 0, 1, 2, 3, 4, 5, 6, 7, 8, 9, 10, 11, 12, 13, 14, 15);
    return __builtin_shufflevector(x, y, 0, 1, 2, 3, 4, 5, 6, 7, 8, 9, 10, 11, 12, 13, 14, 15, 16, 17, 18, 19, 20, 21, 22, 23, 24, 25, 26, 27, 28, 29, 30, 31);
}

DEV void role_chain(const u2 tag, const int role, const Params& p, LAS char* L, const int c, rsrc_t rw) {
    asm volatile("" : "+s"(rw));
    const int lane = threadIdx.x & 63, wave = __builtin_amdgcn_readfirstlane(threadIdx.x >> 6);
    const int h = c >> 1, half = c & 1;
    const unsigned lbase = (unsigned)(size_t)L;
    const int G = lane >> 4, q4 = (lane >> 2) & 3, pp = lane & 3, n16 = lane & 15;
    Clk clk = {0u, 0u, 23u * 256u, 186737708u};
    clk_edge(clk);
    unsigned sel32, known;
    u8v W[45];
    {
        const char* gq = (const char*)p.wq + h * 256, * gk = (const char*)p.wk + h * 256, * gv = (const char*)p.wv + h * 256 + half * 128;
        auto issue = [&](int ks) {
            const unsigned slot = lbase + (ks & 3) * 40960;
#pragma unroll
            for (int i = 0; i < 4; ++i) {
                const int d = wave * 16 + i * 4 + (lane >> 4);
                const size_t off = (size_t)(64 * ks + d) * 8192 + (((lane & 15) ^ (d & 14)) * 16);
                glds16(gq + off, slot + (wave * 4 + i) * 1024);
                glds16(gk + off, slot + 16384 + (wave * 4 + i) * 1024);
            }
#pragma unroll
            for (int i = 0; i < 2; ++i) {
                const int rho = wave * 16 + i * 8 + (lane >> 3);
                const int d = (rho & ~3) | ((rho & 1) << 1) | ((rho >> 1) & 1);
                const size_t off = (size_t)(64 * ks + d) * 8192 + (((lane & 7) ^ (((d >> 2) & 3) * 2)) * 16);
                glds16(gv + off, slot + 32768 + (wave * 2 + i) * 1024);
            }
        };
        auto issue_wo = [&](int part) {
            const char* go = (const char*)p.wo + (size_t)(h * 128 + half * 64) * 1152;
#pragma unroll
            for (int i = 0; i < 10; ++i) {
                const int ins = part * 36 + wave * 9 + (i < 9 ? i : 8);
                const int u = ins * 64 + lane, e = u / 72, cc = u % 72;
                glds16(go + (size_t)e * 1152 + ((cc ^ (((e >> 4) & 1) * 4)) * 16), lbase + 81920 + ins * 1024);
            }
        };
        issue(0); issue(1); issue(2); issue(3);
        const int p_ = q4 >> 1, b_ = q4 & 1;
        const int dl = 16 * (G >> 1) + 8 * p_ + 4 * (G & 1) + 2 * b_;
        const int aq0 = dl * 256 + (((4 * wave + (pp >> 1)) ^ (dl & 14)) * 16) + (pp & 1) * 8, aq1 = aq0 ^ 32;
        const int rl = 16 * (G >> 1) + 8 * p_ + 4 * (G & 1) + b_;
        const int av = 32768 + rl * 128 + (((2 * wave + (pp >> 1)) ^ (((dl >> 2) & 3) * 2)) * 16) + (pp & 1) * 8;
        sfor<9>([&](auto KS) {
            constexpr int ks = KS;
            if constexpr (ks <= 5) VM_WAIT(30); else if constexpr (ks == 6) VM_WAIT(20); else if constexpr (ks == 7) VM_WAIT(20); else VM_WAIT(20);
            BARRIER();
            LAS char* S = L + (ks & 3) * 40960;
#pragma unroll
            for (int t = 0; t < 4; ++t) {
                LAS char* a = S + (t >> 1) * 16384 + ((t & 1) ? aq1 : aq0);
                W[ks * 4 + t] = cat(cat(trd(a), trd(a + 256)), cat(trd(a + 8192), trd(a + 8192 + 256)));
            }
            { LAS char* a = S + av; W[36 + ks] = cat(cat(trd(a), trd(a + 256)), cat(trd(a + 4096), trd(a + 4096 + 256))); }
            BARRIER();
            if constexpr (ks + 4 < 9) issue(ks + 4); else if constexpr (ks == 6) issue_wo(0); else if constexpr (ks == 7) issue_wo(1);
        });
        sel32 = wait_go(rw, tag);
        BARRIER();
        const int el = 16 * (G & 1) + 4 * (G >> 1) + b_;
#pragma unroll 1
        for (int f = wave; f < 36; f += 4) {
            const int dt = f >> 1, ks = f & 1;
            LAS char* a = L + 81920 + (32 * ks + el) * 1152 + (((4 * dt + 2 * p_ + (pp >> 1)) ^ ((G & 1) * 4)) * 16) + (pp & 1) * 8;
            const u4 lo = cat(trd(a), trd(a + 2 * 1152)), hi = cat(trd(a + 8 * 1152), trd(a + 10 * 1152));
            *(LAS u4*)(L + C_WO + f * 2048 + lane * 16) = lo; *(LAS u4*)(L + C_WO + f * 2048 + 1024 + lane * 16) = hi;
        }
        BARRIER();
#pragma unroll
        for (int i = 0; i < 5; ++i) {
            *(LAS u4*)(L + C_WV + wave * 10240 + i * 2048 + lane * 16) = __builtin_shufflevector(W[40 + i], W[40 + i], 0, 1, 2, 3);
            *(LAS u4*)(L + C_WV + wave * 10240 + i * 2048 + 1024 + lane * 16) = __builtin_shufflevector(W[40 + i], W[40 + i], 4, 5, 6, 7);
        }
    }
    MARK(p.ws, 1024 + role);
    ST_DECL; ST(0);
    const unsigned sel = sel32 >> 16;
    unsigned sbA = WS_PART + (sel & 1u) * 8192u, sbB = WS_PART + 2 * MB + ((sel >> 1) & 1u) * 8192u, sbC = WS_PART + 4 * MB + ((sel >> 2) & 1u) * 8192u;
    const unsigned sbm = WS_MEM + ((sel >> 3) & 1u) * 8192u;
    const unsigned idx = (lane & 8) ? 0xEEEEu : 0x4444u;
    const unsigned l16 = lane * 16, l16s = l16 ^ ((l16 >> 1) & 128);
    const unsigned vmem = (G * 2 + ((lane >> 2) & 1)) * 128 + (((lane >> 3) & 1) * 4 + (lane & 3)) * 16;
    const unsigned vpart = G * 16384 + ((c ^ G) & 3) * 128 + n16 * 8;
    unsigned spart[5];
#pragma unroll
    for (int j = 0; j < 5; ++j) { const int dt = j < 4 ? 4 * j + wave : 14 + wave; spart[j] = dt * 65536 + (((c >> 2) + dt) & 15) * 512; }
    const float scl = 0.08838834764831845f, l2e = 1.4426950408889634f;
    const unsigned skx = (32 + h) * 16384 + wave * 2048, sqx = h * 16384 + wave * 2048;
    const unsigned svx = (64 + h) * 16384 + (wave >> 1) * 4096 + (half * 2 + (wave & 1)) * 1024;
    const unsigned so_ = wave >= 2 ? pm(LA_O + ((wave & 1) * 128 + c * 2) * 1024) : 0x40000000u;
    const unsigned vflag = (wave >= 2 && lane == 0) ? WS_CTL + (CL_ATT + c * 2 + (wave & 1)) * 128 : OOB;
    const unsigned vcan = threadIdx.x == 0 ? 1024 + c * 64 : OOB;
    LAS char* aqk_w = L + C_QK + (G & 1) * 2176 + (32 * wave + 16 * (lane >> 5) + n16) * 16;
    LAS char* aqk_r = L + C_QK + (pp & 1) * 2176 + (4 * G + q4) * 16;
    LAS char* avm_w = L + C_VM + (wave >> 1) * 1024 + l16 + (wave & 1) * 8;
    LAS char* avm_r = L + C_VM + l16;
    const int vx_r = 16384 + (pp * 16 + ((4 * G + q4) ^ ((pp & 1) * 8))) * 16;
    if (wave == 0) {
        const float ni = -__builtin_inff();
        *(LAS f4*)(L + C_MK + 32 + l16) = (f4){4 * G <= n16 ? 0.f : ni, 4 * G + 1 <= n16 ? 0.f : ni, 4 * G + 2 <= n16 ? 0.f : ni, 4 * G + 3 <= n16 ? 0.f : ni};
        if (lane < 2) *(LAS f4*)(L + C_MK + l16) = lane ? (f4){0.f, 0.f, 0.f, 0.f} : (f4){ni, ni, ni, ni};
    }
    auto opaque_lane = [&]() { int l_; asm volatile("v_mbcnt_lo_u32_b32 %0, -1, 0\n\tv_mbcnt_hi_u32_b32 %0, -1, %0" : "=v"(l_)); return l_; };
    auto prog_wait = [&](unsigned need) {
        const int l_ = opaque_lane(), pn = min(l_, 2) * 16 + (h >> 1);
        return min16(poll_prog(rw, WS_CTL + (CL_QKV + pn) * 128, need));
    };
#define DMA(m0v, vo, so, off) asm volatile("s_mov_b32 m0, %0\n\ts_nop 0\n\tbuffer_load_dwordx4 %1, %2, %3 offen offset:" #off " sc1 lds" :: "s"(m0v), "v"(vo), "s"(rw), "s"(so) : "m0", "memory")
#define MEM_REGS "+{v[220:223]}"(a[0]), "+{v[224:227]}"(a[1]), "+{v[228:231]}"(a[2]), "+{v[232:235]}"(a[3]), "+{v[236:239]}"(a[4]), \
        "+{v[240:243]}"(a[5]), "+{v[244:247]}"(a[6]), "+{v[248:251]}"(a[7]), "+{v[252:255]}"(a[8])
#define STB(val, vo, so, off) asm volatile("buffer_store_dwordx4 %0, %1, %2, %3 offen offset:" #off " sc1" :: "v"(val), "v"(vo), "s"(rw), "s"(so) : "memory")
#define STA(val, vo, so, off) asm volatile("buffer_store_dwordx4 %0, %1, %2, %3 offen offset:" #off " sc1\n\ts_nop 1" :: "v"(val), "v"(vo), "s"(rw), "s"(so) : "memory")
    ST(1);
    known = prog_wait(1);
    { const unsigned mb = lbase + C_KX + wave * 2048, f0 = far_a(sel32, 0), k0 = f0 + skx, q0 = f0 + sqx, v0 = f0 + svx;
      DMA(mb, l16, k0, 0); DMA(mb, l16, k0, 1024); DMA(mb + 8192, l16, q0, 0); DMA(mb + 8192, l16, q0, 1024); DMA(lbase + C_KX + 16384 + wave * 1024, l16s, v0, 0); }
    VM_WAIT(0);
    ST(2); ST_FLUSH(p.ws, role, 65);
    MARK(p.ws, 1280 + role);
    u8v ox = {0u, 0u, 0u, 0u, 0u, 0u, 0u, 0u};
    u32v WW[10];
#pragma unroll
    for (int i = 0; i < 10; ++i) WW[i] = cat4(W[4 * i], W[4 * i + 1], W[4 * i + 2], W[4 * i + 3]);
    const unsigned mkb = lbase + C_MK;
    const u16v cv = {l16, l16s, vmem, vpart, idx, vflag, vcan, (unsigned)(size_t)aqk_w, (unsigned)(size_t)aqk_r, (unsigned)(size_t)avm_w, (unsigned)(size_t)avm_r,
                     (unsigned)vx_r, lbase + C_WV + wave * 10240 + l16, lbase + C_WO + wave * 4096 + l16, (wave & 1) ? mkb + 32 + l16 : mkb, (wave & 1) ? mkb + 16 : mkb + 32 + l16};
    const u2 cv2 = G < 2 ? (u2){0x3f803f80u, 0x3f803f80u} : (u2){0u, 0u};
    const u4 cv3 = {0x3f803f80u, 0x3f803f80u, 0x3f803f80u, 0x3f803f80u};
    const unsigned vcl = wave == 0 ? lane * 64 + 1024 : sbm + 16384u + 5120u;
    clk_edge(clk);
    const unsigned xb = lbase + C_KX, dur = 1500;
    u16v sa = {xb, sbm, 0u, 0x40000000u, 0x40000000u, 0x40000000u, so_ + far_a(sel32, 0), xb + 20480 + wave * 2048, xb + 20480 + 16384 + wave * 1024,
               RFL(dur - clk.ct), RFL(clk.tpc), RFL(clk.rt), wave >= 2 ? 8192u + (wave & 1) * 4096u : 0u, 0u, RFL(clk.ct), RFL(clk.cpt)};
    u8v sb = {sbA + spart[0], sbA + spart[1], sbA + spart[2], sbA + spart[3], sbA + spart[4], RFL(min(known, (unsigned)RING)), 0x3e0293eeu, 0u};
    u4 sc = {SPIN_CAP, 0u, 0u, dur};
    u16v sd = {spart[0], spart[1], spart[2], spart[3], spart[4], sbA, sbB, sbC, 0u, 540u, 2 * xb + 20480, (unsigned)wave * 1024u, so_, (unsigned)(DELAY2 + 9), (unsigned)DTOL, (unsigned)DEXTRA};
    unsigned sx = 144 * MB + (role * 4 + wave) * 66 * 64;
    const u4 se = {skx, svx, sqx, ~sel32};
    u2 sf = {0u, (unsigned)DBUMP};
#define CHAIN_TXT CHAIN_STEP
    asm volatile(CHAIN_TXT
                 : "+{v[232:239]}"(ox), "+{s[36:51]}"(sa), "+{s[52:59]}"(sb), "+{s[60:63]}"(sc), "+{s[64:79]}"(sd), "+{s88}"(sx), "+{s[84:85]}"(sf)
                 : "{a[0:31]}"(WW[0]), "{a[32:63]}"(WW[1]), "{a[64:95]}"(WW[2]), "{a[96:127]}"(WW[3]), "{a[128:159]}"(WW[4]), "{a[160:191]}"(WW[5]), "{a[192:223]}"(WW[6]),
                   "{a[224:255]}"(WW[7]), "{v[24:55]}"(WW[8]), "{v[56:87]}"(WW[9]), "{v[8:23]}"(cv), "{v[6:7]}"(cv2), "{v5}"(vcl), "{v[244:247]}"(cv3), "{s[80:83]}"(se), [rs] "s"(rw)
                 : "memory", "m0", "vcc", "scc", "v2", "v3", "v4", CLOB_S, CLOB_V);
    if (__builtin_expect(sc[0] == 0u, 0)) st4(rw, 1u, WS_CTL + CL_ABORT * 128, 0);
    if (wave >= 2) {
        VM_WAIT(0);
        st4(rw, (unsigned)NSEG, cv[5], 0);
    }
    __builtin_amdgcn_endpgm();
}

DEV void role_reduce(const u2 tag, const int role, const Params& p, const int rc, rsrc_t rw) {
    asm volatile("" : "+s"(rw));
    const int lane = threadIdx.x & 63, wave = __builtin_amdgcn_readfirstlane(threadIdx.x >> 6), n16 = lane & 15;
    if (wave == 3) touch_blocks(rw);
    if (wave * 31 + rc >= 72 || (rc == 31 && wave > 0)) return;
    const unsigned sel32 = wait_go(rw, tag), sel = sel32 >> 16;
    if (rc == 31) {
        const unsigned vo = WS_CTL + ((lane < 48 || p.rolemask != 0xffu) ? CL_QKV + lane % 48 : CL_R1) * 128, gb = WS_MEM + ((sel >> 3) & 1u) * 8192u + 16384u + 5120u;
        unsigned done = 0, spins = 0;
        while (done < (unsigned)NSEG) {
            CBAR();
            const unsigned v = ld4(rw, vo, 0), kn = min64(lane < 48 ? v : v + RING);
            if (kn > done) { if (lane == 0) st4(rw, kn, 0u, gb); done = kn; spins = 0; }
            if (spin_out(spins, rw)) break;
            if (done < 8u) __builtin_amdgcn_s_sleep(2); else __builtin_amdgcn_s_sleep(12);
        }
        return;
    }
    unsigned sbA = WS_PART + (sel & 1u) * 8192u, sbB = WS_PART + 2 * MB + ((sel >> 1) & 1u) * 8192u, sbC = WS_PART + 4 * MB + ((sel >> 2) & 1u) * 8192u;
    unsigned som = WS_MEM + ((sel >> 3) & 1u) * 8192u;
    const unsigned l16 = lane * 16, one = (lane & 1) ? 0x3f800000u : 0x00003f80u; const int w_ = n16 >> 1;
    const u4 bsel = {w_ == 0 ? one : 0u, w_ == 1 ? one : 0u, w_ == 2 ? one : 0u, w_ == 3 ? one : 0u};
    const unsigned vout = (lane & 8) ? OOB : (((lane >> 4) & 1) * 4 + (lane & 3)) * 16 + ((lane >> 5) * 2 + ((lane >> 2) & 1)) * 4;
    const int task = wave * 31 + rc;
    Clk clk = {0u, 0u, 23u * 256u, 186737708u};
    clk_edge(clk); __builtin_amdgcn_s_sleep(40); clk_edge(clk);
    u16v sa = {0u, som, 0u, 0u, 0u, 0u, RFL(clk.rt), RFL(clk.ct), 0u, 0u, 0u, RFL(clk.rt), 0u, 0u, RFL(clk.ct), RFL(clk.cpt)};
    u8v sb = {0u, 0u, 0u, 0u, 0u, VETO, 0u, (unsigned)(DELAY1 + 3 + ((task >> 4) - 3) * DSTAG)};
    u2 sf = {0u, (unsigned)DBUMP};
    u4 sc = {SPIN_CAP, 0u, 0u, 0u};
    u16v sd = {(unsigned)task * 16384u, 32768u + (task >> 6) * 16384u + (task & 63) * 128u, (unsigned)(DELAY1 + ((task >> 4) - 3) * DSTAG), 0u, 0u, sbA, sbB, sbC, 0u, 540u,
               (unsigned)VLEAD, (unsigned)RESETAT, 0u, (unsigned)(DELAY2 + 9), (unsigned)DTOL, (unsigned)DEXTRA};
    const u4 sv = {SENT, SENT, SENT, SENT};
    unsigned sx = 146 * MB + task * 64 * 16;
#define RED_TXT RED_LOOP
    asm volatile(RED_TXT : "+{s[36:51]}"(sa), "+{s[52:59]}"(sb), "+{s[60:63]}"(sc), "+{s[64:79]}"(sd), "+{s88}"(sx), "+{s[84:85]}"(sf)
                 : "{v5}"(lane * 64 + 1024), "{v6}"(l16), "{v7}"(vout), "{v[8:11]}"(bsel), "{v[12:15]}"(sv), "{v3}"(1024u + (((lane >> 1) % 3) * 22 + (task % 10) * 2 + (lane & 1)) * 64u), [rs] "s"(rw)
                 : "memory", "vcc", "scc", CLOB_S, "v4", "v16", "v17", "v18", "v19", "v20", "v21", "v22", "v23", "v24", "v25", "v26", "v27", "v28", "v29", "v30", "v31", "v32", "v33", "v34", "v35", "v36", "v37", "v38", "v39", "v40", "v41", "v42", "v43", "v44", "v45", "v46", "v47", "v48", "v49", "v50", "v51", "v52", "v53");
    if (__builtin_expect(sc[0] == 0u, 0)) st4(rw, 1u, WS_CTL + CL_ABORT * 128, 0);
}

template <int I> DEV void mfw(f4& d, const u4& w, const u4& x) {
    if constexpr (I < 64) asm volatile(AL8 "v_mfma_f32_16x16x32_bf16 %0, %1, %2, %0" : "+v"(d) : "a"(w), "v"(x));
    else asm volatile(AL8 "v_mfma_f32_16x16x32_bf16 %0, %1, %2, %0" : "+v"(d) : "v"(w), "v"(x));
}
DEV void mfwz(f4& d, const u4& w, const u4& x) { asm volatile(AL8 "v_mfma_f32_16x16x32_bf16 %0, %1, %2, 0" : "=&v"(d) : "a"(w), "v"(x)); }
DEV void mfwc(f4& d, const u4& w, const u4& x, const f4& c) { asm volatile(AL8 "v_mfma_f32_16x16x32_bf16 %0, %1, %2, %3" : "=&v"(d) : "a"(w), "v"(x), "v"(c)); }
DEV float vmul(float a, float b) { float r; asm("v_mul_f32 %0, %1, %2" : "=v"(r) : "v"(a), "v"(b)); return r; }
DEV u4 pack8(const f4& a0, const f4& b0, float k) {
    const f4 a = {vmul(a0[0], k), vmul(a0[1], k), vmul(a0[2], k), vmul(a0[3], k)}, b = {vmul(b0[0], k), vmul(b0[1], k), vmul(b0[2], k), vmul(b0[3], k)};
    return (u4){pk_bf16(a[0], a[1]), pk_bf16(a[2], a[3]), pk_bf16(b[0], b[1]), pk_bf16(b[2], b[3])}; }

template <int OFF> DEV void xdm(unsigned m0v, unsigned vo, rsrc_t rw, unsigned so) {
    asm volatile("s_mov_b32 m0, %0\n\ts_nop 0\n\tbuffer_load_dwordx4 %1, %2, %3 offen offset:%c4 sc1 lds" :: "s"(m0v), "v"(vo), "s"(rw), "s"(so), "i"(OFF) : "m0", "memory");
}
DEV void tie2(f4& a, f4& b) { asm volatile("" : "+v"(a), "+v"(b)); }
DEV void role_qkv(const u2 tag, const int role, const Params& p, LAS char* L, const int P, rsrc_t rw) {
    asm volatile("" : "+s"(rw));
    const int lane = threadIdx.x & 63, wave = __builtin_amdgcn_readfirstlane(threadIdx.x >> 6);
    const unsigned lbase = (unsigned)(size_t)L, l16 = lane * 16;
    const int G = lane >> 4, q4 = (lane >> 2) & 3, pp = lane & 3;
    u4 W[72];
    unsigned sel = 0;
#define XDM(m0v, so, off) asm volatile("s_mov_b32 m0, %0\n\ts_nop 0\n\tbuffer_load_dwordx4 %1, %2, %3 offen offset:" #off " sc1 lds" :: "s"(m0v), "v"(l16), "s"(rw), "s"(so) : "m0", "memory")
    auto slot_off = [&](int s) { return (unsigned)((s + 3) & 3) * 36864u; };
    auto issue_x = [&](int s) {
        const unsigned so = WS_XN + (unsigned)min(s, NSEG - 1) * 36864u + wave * 9216, m = lbase + slot_off(s) + wave * 9216;
        XDM(m, so, 0); XDM(m, so, 1024); XDM(m, so, 2048); XDM(m, so, 3072);
        XDM(m + 4096, so + 4096, 0); XDM(m + 4096, so + 4096, 1024); XDM(m + 4096, so + 4096, 2048); XDM(m + 4096, so + 4096, 3072); XDM(m + 8192, so + 8192, 0);
    };
    {
        const unsigned short* w = P < 16 ? p.wq : P < 32 ? p.wk : p.wv;
        const char* src[4];
#pragma unroll
        for (int i = 0; i < 4; ++i) { const int r = wave * 8 + i * 2 + (lane >> 5); src[i] = (const char*)(w + (size_t)r * 4096 + (P & 15) * 256) + (((lane & 31) ^ ((r & 7) * 2)) * 16); }
        auto issue = [&](int un) {
#pragma unroll
            for (int i = 0; i < 4; ++i) glds16(src[i] + (size_t)un * 32 * 8192, lbase + (un % 6) * 16384 + (wave * 4 + i) * 1024);
        };
        issue(0); issue(1); issue(2); issue(3); issue(4); issue(5);
        const int rl = 4 * G + q4;
        int ar[4];
#pragma unroll
        for (int t = 0; t < 4; ++t) ar[t] = rl * 512 + (((8 * wave + 2 * t + (pp >> 1)) ^ ((rl & 7) * 2)) * 16) + (pp & 1) * 8;
        sfor<9>([&](auto U2) {
            constexpr int un = 2 * U2;
            if constexpr (un <= 12) VM_WAIT(16); else VM_WAIT(0);
            BARRIER();
#pragma unroll
            for (int k2 = 0; k2 < 2; ++k2)
#pragma unroll
                for (int t = 0; t < 4; ++t) { LAS char* a = L + ((un + k2) % 6) * 16384 + ar[t]; W[(un + k2) * 4 + t] = cat(trd(a), trd(a + 16 * 512)); }
            BARRIER();
            if constexpr (un + 6 < 18) { issue(un + 6); issue(un + 7); }
            if constexpr (un == 12) { sel = get_sel(rw, tag); poll_prog<4>(rw, WS_CTL + (CL_XN + lane) * 128, 1); issue_x(0); }
        });
    }
    issue_x(1); issue_x(2);
    MARK(p.ws, 1024 + role);
    ST_DECL; ST(0); ST_FLUSH(p.ws, role, 65);
    const unsigned sst = (P * 2 + (wave >> 1)) * 16384 + (wave & 1) * 2048;
    unsigned gate = p.rolemask == 0xffu ? 0u : 64u;
    const unsigned vflag = threadIdx.x == 0 ? WS_CTL + (CL_QKV + P) * 128 : OOB;
    const float qs = P < 16 ? 0.08838834764831845f * 1.4426950408889634f : 1.0f;
    constexpr int PD = 3;
    u4 xf[PD + 1][2];
    f4 acc[2][2][4];
#pragma unroll
    for (int i = 0; i < PD; ++i) { LAS char* S = L + slot_off(0) + l16; xf[i][0] = *(LAS u4*)(S + i * 1024); xf[i][1] = *(LAS u4*)(S + 18432 + i * 1024); }
    auto seg = [&](auto PAR, int s) {
        constexpr int par = PAR;
        LAS char* S = L + slot_off(s) + l16; LAS char* Sn = L + slot_off(s + 1) + l16;
        const unsigned sx = WS_XN + (unsigned)min(s + 3, NSEG - 1) * 36864u + wave * 9216, mx = lbase + slot_off(s + 3) + wave * 9216;
        const unsigned so = s > 0 ? sst + far_a(sel, s - 1) : 0x40000000u;
        if (__builtin_expect(gate + RING <= (unsigned)s, 0)) gate = RFL(poll_prog<40>(rw, WS_CTL + CL_R1 * 128, s + 1 - RING));
        sfor<18>([&](auto KS) {
            constexpr int ks = KS, cur = (ks + 2 * par) % (PD + 1), nx = (ks + PD + 2 * par) % (PD + 1);
            if constexpr (ks + PD == 18) {
                VM_WAIT(9); BARRIER();
                st4(rw, (unsigned)s, vflag, 0);
            }
            if constexpr (ks == 5) { if (s >= 1 && s <= 4) { VM_WAIT(0); BARRIER(); st4(rw, (unsigned)s, vflag, 0); MARK(p.ws, 1792 + (s - 1) * 64 + P); } }
            if constexpr (ks == 0) { mfwz(acc[par][0][0], W[0], xf[cur][0]); mfwz(acc[par][1][0], W[0], xf[cur][1]); }
            else { mfw<ks * 4>(acc[par][0][0], W[ks * 4], xf[cur][0]); mfw<ks * 4>(acc[par][1][0], W[ks * 4], xf[cur][1]); }
            if constexpr (ks + PD < 18) { xf[nx][0] = *(LAS u4*)(S + (ks + PD) * 1024); xf[nx][1] = *(LAS u4*)(S + 18432 + (ks + PD) * 1024); }
            else { xf[nx][0] = *(LAS u4*)(Sn + (ks + PD - 18) * 1024); xf[nx][1] = *(LAS u4*)(Sn + 18432 + (ks + PD - 18) * 1024); }
            SCHED();
            sfor<3>([&](auto T) {
                constexpr int t = T + 1;
                if constexpr (ks == 0) { mfwz(acc[par][0][t], W[t], xf[cur][0]); mfwz(acc[par][1][t], W[t], xf[cur][1]); }
                else { mfw<ks * 4 + t>(acc[par][0][t], W[ks * 4 + t], xf[cur][0]); mfw<ks * 4 + t>(acc[par][1][t], W[ks * 4 + t], xf[cur][1]); }
                if constexpr (t == 1 && ks >= 1 && ks <= 4) {
                    constexpr int rt = (ks - 1) >> 1, pr = (ks - 1) & 1;
                    tie2(acc[par ^ 1][rt][2 * pr], acc[par ^ 1][rt][2 * pr + 1]);
                    st16(rw, pack8(acc[par ^ 1][rt][2 * pr], acc[par ^ 1][rt][2 * pr + 1], qs), l16 + pr * 1024, so + rt * 4096);
                    SCHED();
                }
                if constexpr (t == 1 && ks >= 5 && ks <= 13) {
                    constexpr int i = ks - 5;
                    xdm<(i & 3) * 1024>(mx + (i >> 2) * 4096, l16, rw, sx + (i >> 2) * 4096);
                }
            });
        });
    };
#pragma unroll
    for (int rt = 0; rt < 2; ++rt)
#pragma unroll
        for (int t = 0; t < 4; ++t) acc[1][rt][t] = (f4){0.f, 0.f, 0.f, 0.f};
#pragma unroll 1
    for (int s = 0; s < NSEG; s += 2) {
        ST(0);
        seg(std::integral_constant<int, 0>{}, s);
        seg(std::integral_constant<int, 1>{}, s + 1);
        ST(1); ST_FLUSH(p.ws, role, s);
    }
    asm volatile("s_nop 7" : "+v"(acc[1][0][0]), "+v"(acc[1][0][1]), "+v"(acc[1][0][2]), "+v"(acc[1][0][3]), "+v"(acc[1][1][0]), "+v"(acc[1][1][1]), "+v"(acc[1][1][2]), "+v"(acc[1][1][3]));
#pragma unroll
    for (int i = 0; i < 4; ++i) st16(rw, pack8(acc[1][i >> 1][2 * (i & 1)], acc[1][i >> 1][2 * (i & 1) + 1], qs), l16 + (i & 1) * 1024, sst + far_a(sel, NSEG - 1) + (i >> 1) * 4096);
    VM_WAIT(0);
    BARRIER();
    st4(rw, 2u * NSEG, vflag, 0);
}

template <int KSW>
DEV void role_proj(const u2 tag, const int role, const Params& p, LAS char* L, const unsigned short* wgt, const int kq, const int nj, const rsrc_t rw,
                   const unsigned ws_in, const unsigned ws_out, unsigned vdep, const int cl_out) {
    const int lane = threadIdx.x & 63, wave = __builtin_amdgcn_readfirstlane(threadIdx.x >> 6);
    const unsigned lbase = (unsigned)(size_t)L;
    const int G = lane >> 4, q4 = (lane >> 2) & 3, pp = lane & 3, n16 = lane & 15;
    const int ks0 = (kq * 4 + wave) * KSW;
    u4 W[KSW * 4];
    { const int seg = (KSW - 8) * 36 + nj * 4 + kq; if (seg < NSEG) ln_segment(p, L, rw, seg, tag); }
    const unsigned sel = get_sel(rw, tag);
    const int c8 = lane & 7, rt_r = wave >> 1, nt_r = 2 * (wave & 1) + (c8 >> 2), g_r = c8 & 3;
    const unsigned lout = ws_out + (kq * 32 + rt_r * 16 + (lane >> 3)) * 2304 + nj * 256 + (wave & 1) * 128 + c8 * 16, vout0 = pm(lout), vout1 = pm(lout + 8 * 2304);
#pragma unroll 1
    for (int i = 0; i < RINGS; ++i) { const u4 sv = {SENT, SENT, SENT, SENT}; st16(rw, sv, vout0, far_b(sel, i)); st16(rw, sv, vout1, far_b(sel, i)); }
    VM_WAIT(0);
    BARRIER();
    if (threadIdx.x == 0) st4(rw, 1u, WS_CTL + (CL_ARM + (KSW - 8) * 36 + kq * 9 + nj) * 128, 0);
    for (int i = 0; i < WSLEEP - 1; ++i) __builtin_amdgcn_s_sleep(127);
    {
        const int r0 = lane >> 3;
        const char* src = (const char*)wgt + (size_t)(ks0 * 32 + r0) * 1152 + nj * 128 + (((lane & 7) ^ (((r0 >> 1) & 3) * 2)) * 16);
#pragma unroll
        for (int i = 0; i < KSW * 4; ++i) glds16(src + (size_t)i * 8 * 1152, lbase + wave * 40960 + i * 1024);
        VM_WAIT(0);
        const int rl = 4 * G + q4;
#pragma unroll
        for (int ks = 0; ks < KSW; ++ks)
#pragma unroll
            for (int t = 0; t < 4; ++t) {
                LAS char* a = L + wave * 40960 + ks * 4096 + rl * 128 + (((2 * t + (pp >> 1)) ^ (((rl >> 1) & 3) * 2)) * 16) + (pp & 1) * 8;
                W[ks * 4 + t] = cat(trd(a), trd(a + 2048));
            }
        BARRIER();
    }
    const unsigned l16 = lane * 16;
    unsigned cin[2 * KSW];
#pragma unroll
    for (int i = 0; i < 2 * KSW; ++i) cin[i] = pm(ws_in + ((i & 1) * 16 * KSW + ks0 + (i >> 1)) * 1024);
    auto base_in = [&](int s) { return KSW == 8 ? far_a(sel, s) : far_s(sel, s); };
    const int lw0 = wave * 8192 + G * 256 + ((n16 + 2 * G) & 15) * 16, lw1 = wave * 8192 + G * 256 + ((n16 + 2 * G + 8) & 15) * 16;
    const int lr0 = (rt_r * 4 + nt_r) * 1024 + g_r * 256 + (((lane >> 3) + 2 * c8) & 15) * 16, lr1 = lr0 ^ 128;
    const unsigned varm = WS_CTL + (cl_out + rt_r * 16 + n16) * 128;
    unsigned armed = RINGS;
    u4 a[2][2][KSW];
    auto ld_a = [&](auto PAR, int i, unsigned so) {
        constexpr int par = PAR;
        const int ks = i >> 1, rt = i & 1;
        a[par][rt][ks] = ld16(rw, l16, so + cin[i]);
    };
    auto fetch = [&](auto PAR, int s) {
#pragma unroll
        for (int i = 0; i < 2 * KSW; ++i) ld_a(PAR, i, base_in(s));
        SCHED();
    };
    unsigned pv = poll_prog(rw, vdep, 1);
    fetch(std::integral_constant<int, 0>{}, 0);
    auto seg = [&](auto PAR, auto PRE, int s) {
        constexpr int par = PAR; constexpr bool pre = PRE;
        ST_DECL; ST(0);
        pv = ld4(rw, vdep, 0);
        ST(1);
        const unsigned son = base_in(s + 1), slot = far_s(sel, s);
        f4 acc[2][4];
        sfor<KSW>([&](auto KS) {
            constexpr int ks = KS;
#pragma unroll
            for (int rt = 0; rt < 2; ++rt) {
#pragma unroll
                for (int t = 0; t < 4; ++t) { if constexpr (ks == 0) mfwz(acc[rt][t], W[t], a[par][rt][0]); else mfw<0>(acc[rt][t], W[ks * 4 + t], a[par][rt][ks]); }
                if constexpr (pre) { ld_a(std::integral_constant<int, par ^ 1>{}, 2 * ks + rt, son); SCHED(); }
            }
        });
        ST(2);
        LAS char* R = L + par * 32768;
        tie2(acc[0][0], acc[0][1]); tie2(acc[0][2], acc[0][3]);
#pragma unroll
        for (int t = 0; t < 4; ++t) *(LAS f4*)(R + t * 1024 + ((t & 1) ? lw1 : lw0)) = acc[0][t];
        asm volatile("s_nop 3" ::: "memory"); tie2(acc[1][0], acc[1][1]); tie2(acc[1][2], acc[1][3]);
#pragma unroll
        for (int t = 0; t < 4; ++t) *(LAS f4*)(R + 4096 + t * 1024 + ((t & 1) ? lw1 : lw0)) = acc[1][t];
        if (__builtin_expect(armed <= (unsigned)s, 0)) armed = min16(poll_prog(rw, varm, s + 1));
        BARRIER();
#pragma unroll
        for (int hf = 0; hf < 2; ++hf) {
            LAS char* a_ = R + (hf ? lr1 : lr0);
            const f4 v = (*(LAS f4*)a_ + *(LAS f4*)(a_ + 8192)) + (*(LAS f4*)(a_ + 16384) + *(LAS f4*)(a_ + 24576));
            st16(rw, (__builtin_bit_cast(u4, v) & ~1u) | (unsigned)((s / RINGS) & 1), hf ? vout1 : vout0, slot);
        }
        ST(3);
        if constexpr (!pre) {
            if (s + 1 < NSEG) { if (!__all(pv >= (unsigned)s + 2u)) pv = poll_prog(rw, vdep, s + 2); fetch(std::integral_constant<int, par ^ 1>{}, s + 1); }
        }
        ST(4); ST_FLUSH(p.ws, role, s);
    };
    auto step = [&](auto PAR, int s) {
        if (KSW == 9 && s == SPLIT - 1) { if (lane & 8) vdep += 96 * 128; pv = 0u; }
        if (__all(pv >= (unsigned)s + 2u)) seg(PAR, std::true_type{}, s); else seg(PAR, std::false_type{}, s); };
#pragma unroll 1
    for (int s = 0; s < NSEG; s += 2) { step(std::integral_constant<int, 0>{}, s); step(std::integral_constant<int, 1>{}, s + 1); }
}

template <int OFF> DEV void sdm(unsigned m0v, unsigned vo, rsrc_t rw, unsigned so) {
    asm volatile("s_mov_b32 m0, %0\n\ts_nop 0\n\tbuffer_load_dwordx4 %1, %2, %3 offen offset:%c4 sc1 lds" :: "s"(m0v), "v"(vo), "s"(rw), "s"(so), "i"(OFF) : "m0", "memory");
}
DEV float vadd(float a, float b) { float r; asm("v_add_f32 %0, %1, %2" : "=v"(r) : "v"(a), "v"(b)); return r; }
DEV float vfma(float a, float b, float c) { float r; asm("v_fma_f32 %0, %1, %2, %3" : "=v"(r) : "v"(a), "v"(b), "v"(c)); return r; }
DEV void tie3(f4& a, f4& b, f4& c) { asm volatile("" : "+v"(a), "+v"(b), "+v"(c)); }
DEV void role_ffn1(const u2 tag, const int role, const Params& p, LAS char* L, const int fb, rsrc_t rw, const bool helper) {
    asm volatile("" : "+s"(rw));
    const int lane = threadIdx.x & 63, wave = __builtin_amdgcn_readfirstlane(threadIdx.x >> 6);
    const unsigned lbase = (unsigned)(size_t)L;
    const int G = lane >> 4, q4 = (lane >> 2) & 3, pp = lane & 3, n16 = lane & 15;
    u4 W[54];
    if (!helper && fb == 23) {
        for (int i = threadIdx.x; i < CL_END; i += 256) st4(rw, 0u, WS_CTL + i * 128, 0);
        if (threadIdx.x < 32) st4(rw, 0u, WS_CTL + CL_SEL * 128 + threadIdx.x * 4, 0);
        VM_WAIT(0);
        BARRIER();
        if (threadIdx.x == 0) st8(rw, tag, WS_CTL + CL_INIT * 128, 0);
        MARK(p.ws, 100);
    }
    if (wave == 3) touch_blocks(rw);
    if (!helper && fb < 16 && wave < 2) probe_block(rw, wave * 16 + fb, tag);
    if (!helper) MARK(p.ws, 64 + fb);
    const unsigned sel = get_sel(rw, tag);
    if (!helper) {
        const int q = wave * 24 + fb;
        const unsigned som = WS_MEM + ((sel >> 19) & 1u) * 8192u;
        const u4 sv = {SENT, SENT, SENT, SENT};
        if (q < 72) {
            const unsigned short* src = p.mem0 + (4 * (q & 1) + (lane & 3)) * 576 + 16 * (q >> 1) + 8 * ((lane >> 2) & 1);
            u4 m0 = {src[0] | ((unsigned)src[2] << 16), src[1] | ((unsigned)src[3] << 16), src[4] | ((unsigned)src[6] << 16), src[5] | ((unsigned)src[7] << 16)};
#pragma unroll 1
            for (int b = 0; b < 3; ++b)
#pragma unroll
                for (int i = 0; i < 8; ++i) st16(rw, sv, lane * 16 + i * 1024, WS_PART + b * 2 * MB + ((sel >> (16 + b)) & 1u) * 8192u + q * 16384);
            m0 = (u4){min(m0.x, SENT - 1), min(m0.y, SENT - 1), min(m0.z, SENT - 1), min(m0.w, SENT - 1)};
#pragma unroll
            for (int i = 0; i < 8; ++i) st16(rw, (i == 0 && lane < 8) ? m0 : sv, (i * 8 + (lane >> 3)) * 32768u + (lane & 7) * 16, som + (q >> 6) * 16384u + (q & 63) * 128u);
        } else {
#pragma unroll 1
            for (int t = q - 72; t < NSEG; t += 24) { st4(rw, SENT, lane * 64, som + 17408u + t * 32768u); if (lane < 2) st4(rw, lane ? SENT : 0u, lane * 256u, som + 21504u + t * 32768u); }
        }
        VM_WAIT(0);
        if (lane == 0) st4(rw, 1u, WS_CTL + (CL_RED + q) * 128, 0);
        if (q == 95) {
            poll_prog<2>(rw, WS_CTL + (CL_RED + lane) * 128, 1); poll_prog<2>(rw, WS_CTL + (CL_RED + 64 + (lane & 31)) * 128, 1);
            if (lane == 0) st8(rw, (u2){tag.x ^ tag.y, sel}, WS_CTL + CL_GO8 * 128, 0);
        }
    }
    for (int i = 0; i < WSLEEP - 1 && !helper; ++i) __builtin_amdgcn_s_sleep(127);
    int tl[3];
#pragma unroll
    for (int t = 0; t < 3; ++t) tl[t] = (wave & 1) ? (t + 1) % 3 : t;
#pragma unroll
    for (int hf = 0; hf < 2; ++hf) {
#pragma unroll
        for (int i = 0; i < 27; ++i) {
            const int u = i * 64 + lane, r = u / 6, cc = u % 6;
            glds16((const char*)p.w1 + (size_t)(hf * 288 + r) * 9216 + fb * 384 + wave * 96 + cc * 16, lbase + wave * 40960 + i * 1024);
        }
        VM_WAIT(0);
#pragma unroll
        for (int ks = 0; ks < 9; ++ks)
#pragma unroll
            for (int t = 0; t < 3; ++t) {
                LAS char* a = L + wave * 40960 + (ks * 32 + 8 * G + q4) * 96 + tl[t] * 32 + pp * 8;
                W[(hf * 9 + ks) * 3 + t] = cat(trd(a), trd(a + 4 * 96));
            }
        asm volatile("s_waitcnt lgkmcnt(0)" ::: "memory");
    }
    BARRIER();
    f4 bs[3];
#pragma unroll
    for (int t = 0; t < 3; ++t)
#pragma unroll
        for (int i = 0; i < 4; ++i) bs[t][i] = bf_lo(p.b1[fb * 192 + wave * 48 + tl[t] * 16 + 4 * G + i]);
    unsigned vsrc[9];
#pragma unroll
    for (int i = 0; i < 9; ++i) { const int u = i * 64 + lane, row = u / 72, cc = u % 72; vsrc[i] = pm(LB_HN + (wave * 8 + row) * 1152 + ((cc ^ ((wave * 8 + row) >> 1 & 7)) * 16)); }
    const int lr0 = n16 * 1152 + ((G ^ (n16 >> 1)) * 16), lr1 = n16 * 1152 + (((4 + G) ^ (n16 >> 1)) * 16);
    const unsigned l16 = lane * 16, vdep = WS_CTL + (CL_R1 + wave * 8 + (lane & 7)) * 128;
    const int ctp = fb * 12 + wave * 3 + (wave & 1), cts = fb * 12 + wave * 3 + ((wave & 1) ? 0 : 2);
    const unsigned sp0 = pm(LB_H1 + (ctp >> 1) * 1024), sp1 = pm(LB_H1 + 147456 + (ctp >> 1) * 1024);
    const unsigned ss0 = pm(LB_H1 + (cts >> 1) * 1024) + (cts & 1) * 8, ss1 = pm(LB_H1 + 147456 + (cts >> 1) * 1024) + (cts & 1) * 8;
    unsigned gate = RINGS;
    const unsigned vflag = lane == 0 ? WS_CTL + (CL_F1 + (helper ? 96 : 0) + fb * 4 + wave) * 128 : OOB;
    auto fetch = [&](int s) {
        const unsigned m = lbase + (s % 3) * 36864 + wave * 9216, so = far_b(sel, s);
#pragma unroll
        for (int i = 0; i < 9; ++i) sdm<0>(m + i * 1024, vsrc[i], rw, so);
    };
    const float k1 = -0.1029432397f, k0 = -2.302208198f;
    unsigned owed = 0x40000000u;
    const int s0 = helper ? SPLIT : 0;
    unsigned pv = poll_prog<8>(rw, vdep, s0 + 1);
    fetch(s0); VM_WAIT(0);
    auto seg = [&](auto PRE, auto NC, int s) {
        constexpr bool pre = PRE; constexpr int nc = NC;
        ST_DECL; ST(0);
        LAS char* S = L + (s % 3) * 36864 + (nc == 3 && helper ? 18432 : 0);
        const unsigned spa = nc == 3 && helper ? sp1 : sp0, ssa = nc == 3 && helper ? ss1 : ss0;
        BARRIER();
        ST(1);
        constexpr int PD = 6;
        u4 xf[36];
        const unsigned mn = lbase + ((s + 1) % 3) * 36864 + wave * 9216, son = far_b(sel, s + 1), slot = far_s(sel, s);
#define RDX(i) xf[i] = *(LAS u4*)(S + ((i) >= 18 ? 18432 : 0) + (((i) % 18) >> 1) * 128 + (((i) & 1) ? lr1 : lr0))
#pragma unroll
        for (int i = 0; i < PD; ++i) RDX(i);
        if (__builtin_expect(gate <= (unsigned)s, 0)) gate = RFL(poll_prog(rw, WS_CTL + (CL_RDY + 32) * 128, s + 1));
        ST(2);
        f4 acc[6][2]; unsigned og[12]; float g[4], t[4];
        auto stage = [&](auto C, auto K) {
            constexpr int c = C, k = K;
            if constexpr (k == 0) { tie2(acc[c][0], acc[c][1]); g[0] = vadd(acc[c][0][0], acc[c][1][0]); g[1] = vadd(acc[c][0][1], acc[c][1][1]); g[2] = vadd(acc[c][0][2], acc[c][1][2]); }
            if constexpr (k == 1) { g[3] = vadd(acc[c][0][3], acc[c][1][3]); t[0] = vmul(g[0], g[0]); t[1] = vmul(g[1], g[1]); }
            if constexpr (k == 2) { t[2] = vmul(g[2], g[2]); t[3] = vmul(g[3], g[3]); t[0] = vfma(t[0], k1, k0); }
            if constexpr (k == 3) { t[1] = vfma(t[1], k1, k0); t[2] = vfma(t[2], k1, k0); t[3] = vfma(t[3], k1, k0); }
            if constexpr (k == 4) { t[0] = vmul(t[0], g[0]); t[1] = vmul(t[1], g[1]); t[2] = vmul(t[2], g[2]); }
            if constexpr (k == 5) { t[0] = __builtin_amdgcn_exp2f(t[0]); t[3] = vmul(t[3], g[3]); }
            if constexpr (k >= 6 && k <= 8) { t[k - 5] = __builtin_amdgcn_exp2f(t[k - 5]); t[k - 6] = vadd(t[k - 6], 1.0f); }
            if constexpr (k == 9) { t[0] = __builtin_amdgcn_rcpf(t[0]); t[3] = vadd(t[3], 1.0f); }
            if constexpr (k >= 10 && k <= 12) { t[k - 9] = __builtin_amdgcn_rcpf(t[k - 9]); t[k - 10] = vmul(t[k - 10], g[k - 10]); }
            if constexpr (k == 13) { og[2 * c] = pk_bf16(t[0], t[1]); t[3] = vmul(t[3], g[3]); og[2 * c + 1] = pk_bf16(t[2], t[3]); }
            if constexpr (k == 14 && c % 3 == 1) st16(rw, (u4){og[2 * c - 2], og[2 * c - 1], og[2 * c], og[2 * c + 1]}, l16, slot + (c < 3 ? spa : sp1));
            if constexpr (k == 14 && c % 3 == 2) st8(rw, (u2){og[2 * c], og[2 * c + 1]}, l16, slot + (c < 3 ? ssa : ss1));
        };
        sfor<nc * 18>([&](auto I) {
            constexpr int i = I, c = i / 18, ks = i % 18, rt = c / 3, tt = c % 3;
            if constexpr (ks == 0) mfwc(acc[c][0], W[tt], xf[rt * 18], bs[tt]);
            else if constexpr (ks == 1) mfwz(acc[c][1], W[3 + tt], xf[rt * 18 + 1]);
            else mfw<0>(acc[c][ks & 1], W[ks * 3 + tt], xf[rt * 18 + ks]);
            SCHED();
            if constexpr (c == 0) { if constexpr (ks + PD < 18) RDX(ks + PD); else if constexpr (nc == 6) { RDX(2 * ks - 6); RDX(2 * ks - 5); } }
            if constexpr (nc == 6 && (c == 1 || c == 2) && ks < 3) RDX(27 + 3 * c + ks);
            if constexpr (c > 0 && ks >= 3) stage(std::integral_constant<int, c - 1>{}, std::integral_constant<int, ks - 3>{});
            if constexpr (pre && nc == 6 && c >= 3 && ks < 3) sdm<0>(mn + ((c - 3) * 3 + ks) * 1024, vsrc[(c - 3) * 3 + ks], rw, son);
            if constexpr (pre && nc == 3 && i >= 12 && i < 21) sdm<0>(mn + (i - 12) * 1024, vsrc[i - 12], rw, son);
            if constexpr (i == (nc == 6 ? 36 : 11)) { VM_WAIT(0); st4(rw, (unsigned)s, vflag, owed); }
            if constexpr (i == (nc == 6 ? 73 : 38)) pv = ld4(rw, vdep, 0);
            SCHED();
        });
        asm volatile("s_nop 7");
        sfor<15>([&](auto K) { stage(std::integral_constant<int, nc - 1>{}, K); SCHED(); if constexpr (K >= 5 && K <= 12) asm volatile("s_nop 0"); SCHED(); });
        ST(3);
        owed = pre ? 0u : 0x40000000u;
        if constexpr (pre) VM_WAIT(2);
        else if (__all(pv >= (unsigned)s + 2u)) { fetch(s + 1); VM_WAIT(9); st4(rw, (unsigned)s + 1u, vflag, 0); VM_WAIT(1); }
        else {
            pv = ld4(rw, vdep, 0);
            VM_WAIT(0);
            st4(rw, (unsigned)s + 1u, vflag, 0);
            if (s + 1 < NSEG) { if (!__all(pv >= (unsigned)s + 2u)) pv = poll_prog(rw, vdep, s + 2); fetch(s + 1); VM_WAIT(0); }
        }
        ST(4); ST_FLUSH(p.ws, role, s);
    };
#pragma unroll 1
    for (int s = s0; s < SPLIT; ++s) { if (__all(pv >= (unsigned)s + 2u)) seg(std::true_type{}, std::integral_constant<int, 6>{}, s); else seg(std::false_type{}, std::integral_constant<int, 6>{}, s); }
#pragma unroll 1
    for (int s = SPLIT; s < NSEG; ++s) { if (__all(pv >= (unsigned)s + 2u)) seg(std::true_type{}, std::integral_constant<int, 3>{}, s); else seg(std::false_type{}, std::integral_constant<int, 3>{}, s); }
}

template <bool FIRST>
DEV void role_red(const u2 tag, const int role, const Params& p, const int j, const rsrc_t rw) {
    const int lane = threadIdx.x & 63, wave = __builtin_amdgcn_readfirstlane(threadIdx.x >> 6);
    const int r = j * 4 + wave;
    const bool third = lane < 16;
    touch_blocks(rw);
    unsigned vin[4][3], vh[3], vn[3];
#pragma unroll
    for (int i = 0; i < 3; ++i) {
        const bool on = i < 2 || third;
#pragma unroll
        for (int kq = 0; kq < 4; ++kq) vin[kq][i] = on ? pm((FIRST ? LB_PO : LB_PF) + (kq * 32 + r) * 2304 + i * 1024 + lane * 16) : OOB;
        vh[i] = on ? pm(LB_H + r * 1152 + i * 512 + lane * 8) : OOB; vn[i] = on ? pm(LB_HN + r * 1152 + i * 512 + lane * 8) : OOB;
    }
    const unsigned vflag = lane == 0 ? WS_CTL + (CL_R1 + r) * 128 : OOB, sel = get_sel(rw, tag);
    const unsigned varm = lane == 0 ? WS_CTL + (CL_RDY + (FIRST ? 0 : 32) + r) * 128 : OOB;
    poll_prog<20>(rw, WS_CTL + (CL_ARM + (FIRST ? 0 : 36) + min(lane, 35)) * 128, 1);
    MARK(p.ws, 104 + (FIRST ? 0 : 8) + j);
    float b2[12];
    if (!FIRST) {
#pragma unroll
        for (int i = 0; i < 12; ++i) b2[i] = (i < 8 || third) ? bf_lo(p.b2[(lane + 64 * (i >> 2)) * 4 + (i & 3)]) : 0.f;
    }
    const rsrc_t ro = __builtin_amdgcn_make_buffer_rsrc(p.out, 0, 2048 * 1152, 0x00020000);
    u4 q[4][3];
    auto fetch = [&](unsigned slot) {
        CBAR();
#pragma unroll
        for (int kq = 0; kq < 4; ++kq)
#pragma unroll
            for (int i = 0; i < 3; ++i) q[kq][i] = ld16(rw, vin[kq][i], slot);
        SCHED();
    };
    unsigned hk = FIRST ? RING : 0;
    fetch(far_s(sel, 0));
#pragma unroll 1
    for (int s = 0; s < NSEG; ++s) {
        ST_DECL; ST(0);
        const unsigned slot = far_b(sel, s);
        u2 xr[3];
        if (__builtin_expect(hk <= (unsigned)s, 0)) hk = RFL(poll_prog(rw, WS_CTL + (FIRST ? CL_RDY + 32 + r : CL_R1 + r) * 128, FIRST ? s + 1 - (RING - RINGS) : s + 1)) + (FIRST ? RING - RINGS : 0);
        if (FIRST) { const u2* src = (const u2*)(p.x + (s * 32 + r) * 576); xr[0] = src[lane]; xr[1] = src[lane + 64]; xr[2] = src[third ? lane + 128 : lane]; }
        else { xr[0] = ld8(rw, vh[0], slot); xr[1] = ld8(rw, vh[1], slot); xr[2] = ld8(rw, vh[2], slot); }
        unsigned spins = 0;
        const unsigned want = (s / RINGS) & 1 ? 0xffffffffu : 0u;
        for (;;) {
            u4 m = {0u, 0u, 0u, 0u}, m2 = m;
#pragma unroll
            for (int kq = 0; kq < 4; ++kq) { m |= (q[kq][0] ^ want) | (q[kq][1] ^ want); m2 |= q[kq][2] ^ want; }
            const unsigned bad = ((m.x | m.y) | (m.z | m.w)) | (third ? (m2.x | m2.y) | (m2.z | m2.w) : 0u);
            if (!__any(bad & 1u) || spin_out(spins, rw)) break;
            __builtin_amdgcn_s_sleep(RSLEEP);
            fetch(far_s(sel, s));
        }
        f4 t[3];
#pragma unroll
        for (int i = 0; i < 3; ++i)
            t[i] = (__builtin_bit_cast(f4, q[0][i]) + __builtin_bit_cast(f4, q[1][i])) + (__builtin_bit_cast(f4, q[2][i]) + __builtin_bit_cast(f4, q[3][i]));
        ST(1);
        float v[12];
#pragma unroll
        for (int i = 0; i < 3; ++i) {
            const float xv[4] = {bf_lo(xr[i].x), bf_hi(xr[i].x), bf_lo(xr[i].y), bf_hi(xr[i].y)};
#pragma unroll
            for (int e = 0; e < 4; ++e) v[4 * i + e] = FIRST ? ((i < 2 || third) ? rbf(xv[e] + rbf(t[i][e])) : 0.f) : xv[e] + (t[i][e] + b2[4 * i + e]);
        }
        ST(2);
        if (FIRST) {
            const u2 h0 = {pk_bf16(v[0], v[1]), pk_bf16(v[2], v[3])}, h1 = {pk_bf16(v[4], v[5]), pk_bf16(v[6], v[7])}, h2 = {pk_bf16(v[8], v[9]), pk_bf16(v[10], v[11])};
            layernorm12(v, third);
            st8(rw, (u2){pk_bf16(v[0], v[1]), pk_bf16(v[2], v[3])}, vn[0], slot);
            st8(rw, (u2){pk_bf16(v[4], v[5]), pk_bf16(v[6], v[7])}, vn[1], slot);
            st8(rw, (u2){pk_bf16(v[8], v[9]), pk_bf16(v[10], v[11])}, vn[2], slot);
            st8(rw, h0, vh[0], slot); st8(rw, h1, vh[1], slot); st8(rw, h2, vh[2], slot);
            fetch(far_s(sel, s + 1));
            ST(3);
            VM_WAIT(12);
            st4(rw, (unsigned)s + 1u, vflag, 0);
        } else {
            const unsigned so = (s * 32 + r) * 1152;
            st8(ro, (u2){pk_bf16(v[0], v[1]), pk_bf16(v[2], v[3])}, lane * 8, so);
            st8(ro, (u2){pk_bf16(v[4], v[5]), pk_bf16(v[6], v[7])}, lane * 8 + 512, so);
            st8(ro, (u2){pk_bf16(v[8], v[9]), pk_bf16(v[10], v[11])}, third ? lane * 8 + 1024 : OOB, so);
            fetch(far_s(sel, s + 1));
            ST(3);
        }
        if ((s & 1) == 0) st4(rw, (unsigned)(RINGS + s + 1), varm, 0);
        ST(4); ST_FLUSH(p.ws, role, s);
    }
    if (!FIRST && r == 0 && lane == 0) st8(rw, tag, WS_CTL + CL_EPOCH * 128, 0);
}

__global__ void __launch_bounds__(256, 1) k_main(Params p) {
    __shared__ __attribute__((aligned(16))) char lds[163840];
    LAS char* L = (LAS char*)lds;
    const rsrc_t rw = __builtin_amdgcn_make_buffer_rsrc(p.ws, 0, WS_END, 0x00020000);
    MARK(p.ws, 256 + blockIdx.x);
    u4 code_;
    {
        unsigned long long pc; unsigned len;
        asm volatile("s_getpc_b64 %0\n.Lrmt_here:\n\ts_mov_b32 %1, .Lrmt_end-.Lrmt_here\n\t.subsection 2\n.Lrmt_end:\n\t.subsection 0" : "=s"(pc), "=s"(len));
        const rsrc_t rc_ = __builtin_amdgcn_make_buffer_rsrc((void*)(pc & ~15ull), 0, len, 0x00020000);
        const unsigned o_ = ((blockIdx.x >> 3) * 256 + threadIdx.x) * 16;
        code_ = __builtin_amdgcn_raw_buffer_load_b128(rc_, o_, 0, 0) ^ __builtin_amdgcn_raw_buffer_load_b128(rc_, o_, 131072, 0);
    }
    if (threadIdx.x == 0) {
        const unsigned long long tg = __hip_atomic_load((GAS unsigned long long*)(p.ws + WS_CTL + CL_EPOCH * 128), RLX_AGENT) + 0x9E3779B97F4A7C15ull;
        unsigned r = xcc_id() * 32 + (blockIdx.x >> 3);
        for (int i = 0; i < 256; ++i, r = (r + 1) & 255u)
            if (__hip_atomic_exchange((GAS unsigned long long*)(p.ws + WS_CTL + (CL_CLAIM + r) * 128), tg, RLX_AGENT) != tg) break;
        *(LAS u4*)L = (u4){r, (unsigned)tg, (unsigned)(tg >> 32), 0u};
    }
    BARRIER();
    const u4 rl_ = *(LAS u4*)L;
    const int role = RFL(rl_.x);
    const u2 tag = {RFL(rl_.y), RFL(rl_.z)};
    asm volatile("" :: "v"(code_));
    BARRIER();
    MARK(p.ws, 1536 + role);
    { ST_DECL; ST(0);
      ST_FLUSH(p.ws, role, 64); }
    const int xc = role >> 5, ix = role & 31, lane = threadIdx.x & 63, wave = __builtin_amdgcn_readfirstlane(threadIdx.x >> 6);
    const int kq = xc & 3;
    if (xc < 3) {
        const int nc = xc < 2 ? 22 : 20;
        if (!(p.rolemask & 2u)) {}
        else if (ix < nc) role_chain(tag, role, p, L, xc * 22 + ix, rw);
        else role_reduce(tag, role, p, ix - nc < 10 ? (ix - nc) * 3 + xc : 20 + ix - nc, rw);
    } else if (xc == 3 ? ix < 16 : (ix < 8 || ix >= 26)) {
        if (ix < 16) { role_qkv(tag, role, p, L, xc == 3 ? 32 + ix : kq * 8 + ix, rw); MARK(p.ws, 1280 + role); }
        if (xc > 3 && (ix < 6 || ix >= 26)) role_ffn1(tag, role, p, L, kq * 6 + (ix < 6 ? ix : ix - 26), rw, ix < 6);
    }
    else if (xc == 3) {
        if (ix < 24) { if (p.rolemask & 8u) role_red<true>(tag, role, p, ix - 16, rw); }
        else if (p.rolemask & 64u) role_red<false>(tag, role, p, ix - 24, rw);
    }
    else if (ix < 17) { if (p.rolemask & 4u) role_proj<8>(tag, role, p, L, p.wo, kq, ix - 8, rw, LA_O, LB_PO, WS_CTL + (CL_ATT + (kq * 16 + wave * 4) * 2 + (lane & 7)) * 128, CL_RDY); }
    else { if (p.rolemask & 32u) role_proj<9>(tag, role, p, L, p.w2, kq, ix - 17, rw, LB_H1, LB_PF, WS_CTL + (CL_F1 + (kq * 4 + wave) * 6 + min(lane & 7, 5)) * 128, CL_RDY + 32); }
    MARK(p.ws, 768 + role);
}

extern "C" void kernel_launch(void* const* d_in, const int* in_sizes, int n_in, void* d_out, int out_size, void* d_ws, size_t ws_size,
                              hipStream_t stream) {
    Params p;
    p.x = (const unsigned short*)d_in[0]; p.mem0 = (const unsigned short*)d_in[1]; p.wq = (const unsigned short*)d_in[2];
    p.wk = (const unsigned short*)d_in[3]; p.wv = (const unsigned short*)d_in[4]; p.wo = (const unsigned short*)d_in[5];
    p.w1 = (const unsigned short*)d_in[8]; p.b1 = (const unsigned short*)d_in[9]; p.w2 = (const unsigned short*)d_in[10];
    p.b2 = (const unsigned short*)d_in[11]; p.out = (unsigned short*)d_out;
    p.ws = (char*)(((size_t)d_ws + (2u << 20) - 1) & ~(size_t)((2u << 20) - 1));
    p.rolemask = 0xffu; p.pad0 = 0;
    k_main<<<256, 256, 0, stream>>>(p);
}
```
